```python
import torch

def _softmax(x, axis=-1):
    return torch.softmax(x, dim=axis)
def _gelu(x):
    return torch.nn.functional.gelu(x, approximate="tanh")
def _silu(x):
    return torch.nn.functional.silu(x)
def _sigmoid(x):
    return torch.sigmoid(x)
def _layernorm(x, axis=-1, eps=1e-5):
    m = x.mean(dim=axis, keepdim=True)
    v = x.var(dim=axis, keepdim=True, unbiased=False)
    return (x - m) / torch.sqrt(v + eps)
def _scatter_add(tgt, idx, src, axis=0):
    return tgt.clone().index_add_(axis, idx, src)
def _segment_sum(src, seg, n):
    out = torch.zeros((n,) + src.shape[1:], dtype=src.dtype)
    return out.index_add_(0, seg, src)
def _pad(x, pw):
    flat = tuple(v for pair in reversed(pw) for v in pair)
    return torch.nn.functional.pad(x, flat)

kernel_name = 'gated_mix__linear_attn_block__glu_gated_bilinear__prompt'

def setup_inputs():

    _w = (lambda a: (lambda t: t.to(torch.bfloat16) if t.is_floating_point() else t)(torch.as_tensor(a)))
    def _A_setup():
        B, S, D = 1, 2048, 1024
        return {
            "x": _w((lambda *s: torch.randn(*s, dtype=torch.bfloat16) * 0.05)(B, S, D)),
            "wqkv": _w((lambda *s: torch.randn(*s, dtype=torch.bfloat16) * 0.8)(D, 3 * D)),
            "wo": _w((lambda *s: torch.randn(*s, dtype=torch.bfloat16) * 0.05)(D, D)),
        }
    _A_in = _A_setup()

    def _B_setup():
        B, S, D, F = 1, 2048, 1024, 3584
        return {
            "x": _w((lambda *s: torch.randn(*s, dtype=torch.bfloat16) * 0.05)(B, S, D)),
            "w_gate": _w((lambda *s: torch.randn(*s, dtype=torch.bfloat16) * 1.6)(D, F) * (D ** -0.5)),
            "w_up": _w((lambda *s: torch.randn(*s, dtype=torch.bfloat16) * 1.6)(D, F) * (D ** -0.5)),
            "w_down": _w((lambda *s: torch.randn(*s, dtype=torch.bfloat16) * 1.6)(F, D) * (F ** -0.5)),
        }
    _B_in = _B_setup()

    return {
        "A_x": _A_in["x"],
        "A_wqkv": _A_in["wqkv"],
        "A_wo": _A_in["wo"],
        "B_x": _B_in["x"],
        "B_w_gate": _B_in["w_gate"],
        "B_w_up": _B_in["w_up"],
        "B_w_down": _B_in["w_down"],
        "gate_w": _w((lambda *s: torch.randn(*s, dtype=torch.bfloat16) * 0.05)(1024) * 16.0),
    }

def reference(A_x, A_wqkv, A_wo, B_x, B_w_gate, B_w_up, B_w_down, gate_w):
    def _A_ref(x, wqkv, wo):
        B, S, D = x.shape
        H = 32; Dh = D // H
        qkv = (lambda x, *d: x.permute(*d))((x @ wqkv).reshape(B, S, 3, H, Dh), 2, 0, 3, 1, 4)
        q, k, v = qkv[0], qkv[1], qkv[2]
        phi = lambda t: torch.where(t > 0, t + 1, torch.exp(t)) + 1e-6
        pq, pk = phi(q), phi(k)
        kv = torch.einsum("bhsd,bhse->bhde", pk, v)
        z = torch.einsum("bhsd->bhd", pk)
        num = torch.einsum("bhqd,bhde->bhqe", pq, kv)
        den = torch.einsum("bhqd,bhd->bhq", pq, z)[..., None] + 1e-6
        o = num / den
        return (lambda x, *d: x.permute(*d))(o, 0, 2, 1, 3).reshape(B, S, D) @ wo

    def _B_ref(x, w_gate, w_up, w_down):
        g = torch.einsum("bsd,df->bsf", x, w_gate)
        u = torch.einsum("bsd,df->bsf", x, w_up)
        h = g * u
        return torch.einsum("bsf,fd->bsd", h, w_down)

    x = A_x
    g = _sigmoid((x * gate_w).sum(-1, keepdims=True))
    a = _A_ref(x, A_wqkv, A_wo)
    b = _B_ref(x, B_w_gate, B_w_up, B_w_down)
    return g * a + (1 - g) * b
```

```cpp
#include <hip/hip_runtime.h>
#include <hip/hip_bf16.h>
#include <stdint.h>

typedef __attribute__((ext_vector_type(8))) short s16x8;
typedef __attribute__((ext_vector_type(4))) short s16x4;
typedef __attribute__((ext_vector_type(4))) float f32x4;
typedef __attribute__((ext_vector_type(2))) float f32x2;
typedef __attribute__((ext_vector_type(2))) __bf16 bf16x2;
typedef __attribute__((ext_vector_type(2))) unsigned u32x2;
typedef __attribute__((ext_vector_type(4))) unsigned u32x4;
typedef __attribute__((address_space(3))) s16x4* lds_s16x4_p;
typedef __attribute__((address_space(3))) s16x8* lds_s16x8_p;
typedef __attribute__((address_space(3))) char* lds_char_p;

#define S_ 2048
#define D_ 1024
#define F_ 3584
#define KA_ 4608

#define WS_ABUF 0ull
#define WS_PQ   (WS_ABUF + (size_t)S_ * KA_ * 2)
#define WS_KVP  (WS_PQ + (size_t)S_ * D_ * 2)
#define WS_ZP   (WS_KVP + (size_t)8 * 32 * 1024 * 4)
#define WS_G    (WS_ZP + (size_t)8 * 32 * 32 * 4)
#define WS_CNT  (WS_G + (size_t)S_ * 4)

#define WAIT_V(n) asm volatile("s_waitcnt vmcnt(" #n ")" ::: "memory")
#define WAIT_L(n) asm volatile("s_waitcnt lgkmcnt(" #n ")" ::: "memory")
#define BAR __builtin_amdgcn_s_barrier()
#define SCHED __builtin_amdgcn_sched_barrier(0)

__device__ __forceinline__ float bfr(float v) { return (float)(__bf16)v; }
__device__ __forceinline__ unsigned pack2(float a, float b) {
  f32x2 v = {a, b};
  return __builtin_bit_cast(unsigned, __builtin_convertvector(v, bf16x2));
}
__device__ __forceinline__ float phi_f(float acc) {
  float t = bfr(acc);
  float r = t > 0.f ? bfr(t + 1.0f) : bfr(__expf(t));
  return bfr(r + 1e-6f);
}
__device__ __forceinline__ s16x8 lds_rd128(unsigned addr) { return *(lds_s16x8_p)(size_t)addr; }
__device__ __forceinline__ s16x4 lds_tr(unsigned addr) {
  return __builtin_amdgcn_ds_read_tr16_b64_v4i16((lds_s16x4_p)(size_t)addr);
}
__device__ __forceinline__ s16x8 lds_trfrag(unsigned addr) {
  s16x4 lo = lds_tr(addr), hi = lds_tr(addr + 512);
  return __builtin_shufflevector(lo, hi, 0, 1, 2, 3, 4, 5, 6, 7);
}
#define ARD128(dst, addr, off) asm volatile("ds_read_b128 %0, %1 offset:%2" : "=v"(dst) : "v"(addr), "i"(off) : "memory")
#define ATR(dst, addr, off) asm volatile("ds_read_b64_tr_b16 %0, %1 offset:%2" : "=v"(dst) : "v"(addr), "i"(off) : "memory")
#define ATRF(lo, hi, addr, off) { ATR(lo, addr, off); ATR(hi, addr, (off) + 512); }
#define CAT(lo, hi) __builtin_shufflevector(lo, hi, 0, 1, 2, 3, 4, 5, 6, 7)
__device__ __forceinline__ void glds16(const void* g, unsigned lds_addr) {
  __builtin_amdgcn_global_load_lds((const __attribute__((address_space(1))) unsigned*)g,
                                   (__attribute__((address_space(3))) unsigned*)(size_t)lds_addr, 16, 0, 0);
}
#define MFMA(a, b, c) __builtin_amdgcn_mfma_f32_16x16x32_bf16(a, b, c, 0, 0, 0)

#define G1_ASLOT 32768
#define G1_BBASE 65536
#define G1_BSLOT 20480
#define G1_LDS (G1_BBASE + 4 * G1_BSLOT)

__global__ __launch_bounds__(512, 2) void gemm1_kernel(const __bf16* __restrict__ x, const __bf16* __restrict__ wqkv,
                                                       const __bf16* __restrict__ wg, const __bf16* __restrict__ wu,
                                                       const __bf16* __restrict__ gate_w, char* __restrict__ ws) {
  extern __shared__ __attribute__((aligned(16))) char smem[];
  const unsigned lds0 = (unsigned)(size_t)(lds_char_p)smem;
  const int b = blockIdx.x;
  const int xcd = b & 7, idx = b >> 3, rb = idx & 7, ct = xcd * 4 + (idx >> 3);
  const bool isA = ct < 16;
  const int f0 = isA ? ct * 64 : 1024 + (ct - 16) * 160;
  const int wave = __builtin_amdgcn_readfirstlane(threadIdx.x >> 6);
  const int l = threadIdx.x & 63;
  const int grp = wave >> 2, mw = wave & 3;

  const char* xa = (const char*)x + (size_t)(rb * 256 + wave * 32) * 2048;
  const unsigned voffA = (unsigned)((l >> 3) * 2048 + (((l & 7) ^ (l >> 3)) * 16));
  const int krow = 8 * mw + (l >> 3);
  const int cl = (l & 7) ^ ((((l >> 4) & 1) | ((mw & 1) << 1)) << 1);
  const int rs0 = isA ? 3072 : F_;
  const unsigned voffQ = (unsigned)(krow * rs0 * 2 + cl * 16);
  const unsigned voffF = (unsigned)(krow * F_ * 2 + cl * 16);
  const char* sb0 = isA ? (const char*)(wqkv + ct * 64) : (const char*)(wg + f0);
  const char* sb1 = isA ? (const char*)(wqkv + 1024 + ct * 64) : (const char*)(wu + f0);
  const char* sb2 = isA ? (const char*)(wqkv + 2048 + ct * 64) : (const char*)(wg + f0 + 64);
  const char* sb3 = isA ? (const char*)(wg + f0) : (const char*)(wu + f0 + 64);
  const char* p4 = isA ? (const char*)(wu + f0 + cl * 8)
                       : (cl < 4 ? (const char*)(wg + f0 + 128 + cl * 8) : (const char*)(wu + f0 + 128 + (cl - 4) * 8));
  p4 += (size_t)krow * F_ * 2;
  const size_t pst0 = (size_t)32 * rs0 * 2, pstF = (size_t)32 * F_ * 2;

#define G1_LDA(kt, i) glds16(xa + (size_t)(kt) * 128 + (i) * 16384 + voffA, lds0 + ((kt) & 1) * G1_ASLOT + wave * 4096 + (i) * 1024)
#define G1_BDST(p, s) (lds0 + G1_BBASE + ((p) & 3) * G1_BSLOT + (s) * 4096 + mw * 1024)
#define G1_LDB0(p) glds16(sb0 + (size_t)(p) * pst0 + voffQ, G1_BDST(p, 0))
#define G1_LDB1(p) glds16(sb1 + (size_t)(p) * pst0 + voffQ, G1_BDST(p, 1))
#define G1_LDB2(p) glds16(sb2 + (size_t)(p) * pst0 + voffQ, G1_BDST(p, 2))
#define G1_LDB3(p) glds16(sb3 + (size_t)(p) * pstF + voffF, G1_BDST(p, 3))
#define G1_LDB4(p) glds16(p4 + (size_t)(p) * pstF, G1_BDST(p, 4))

  const int g4 = l >> 4, q4 = (l & 15) >> 2, pp = l & 3;
  unsigned aAddr0 = lds0 + (unsigned)((mw * 64 + (l & 15)) * 128 + ((g4 ^ (l & 7)) * 16));
  unsigned aAddr1 = aAddr0 ^ 64u;
  const int sh = ((q4 >> 1) & 1) | ((g4 & 1) << 1);
  const unsigned bl = lds0 + G1_BBASE + (unsigned)((8 * g4 + q4) * 128 + (pp >> 1) * 16 + (pp & 1) * 8);
  unsigned bA0 = bl + (unsigned)(((2 * grp) ^ sh) * 32);
  unsigned bA1 = bl + (unsigned)(((2 * grp + 1) ^ sh) * 32);
  unsigned bQ0 = bl + 4 * 4096 + (unsigned)(((isA ? 2 * grp : grp) ^ sh) * 32);
  unsigned bQ1 = bl + 4 * 4096 + (unsigned)(((isA ? 2 * grp + 1 : grp + 2) ^ sh) * 32);
  int bdelta = 2 * G1_BSLOT;

  f32x4 acc[4][10];
#pragma unroll
  for (int m = 0; m < 4; ++m)
#pragma unroll
    for (int j = 0; j < 10; ++j) acc[m][j] = f32x4{0.f, 0.f, 0.f, 0.f};
  s16x8 af[2][4];
  s16x4 bl_[5], bh_[5];

#define G1_RDA() { ARD128(af[0][0], aAddr0, 0); ARD128(af[0][1], aAddr0, 2048); ARD128(af[0][2], aAddr0, 4096); ARD128(af[0][3], aAddr0, 6144); \
    ARD128(af[1][0], aAddr1, 0); ARD128(af[1][1], aAddr1, 2048); ARD128(af[1][2], aAddr1, 4096); ARD128(af[1][3], aAddr1, 6144); }
#define G1_RDB_LO(ps) { ATRF(bl_[0], bh_[0], bA0, (ps) * G1_BSLOT); ATRF(bl_[1], bh_[1], bA1, (ps) * G1_BSLOT); \
    ATRF(bl_[2], bh_[2], bA0, (ps) * G1_BSLOT + 4096); ATRF(bl_[3], bh_[3], bA1, (ps) * G1_BSLOT + 4096); \
    ATRF(bl_[4], bh_[4], bA0, (ps) * G1_BSLOT + 8192); }
#define G1_RDB_HI(ps) { ATRF(bl_[0], bh_[0], bA1, (ps) * G1_BSLOT + 8192); ATRF(bl_[1], bh_[1], bA0, (ps) * G1_BSLOT + 12288); \
    ATRF(bl_[2], bh_[2], bA1, (ps) * G1_BSLOT + 12288); ATRF(bl_[3], bh_[3], bQ0, (ps) * G1_BSLOT); \
    ATRF(bl_[4], bh_[4], bQ1, (ps) * G1_BSLOT); }
#define G1_MMA(ks, j0) { WAIT_L(0); SCHED; __builtin_amdgcn_s_setprio(1); \
    _Pragma("unroll") for (int j = 0; j < 5; ++j) { const s16x8 bfj = CAT(bl_[j], bh_[j]); _Pragma("unroll") for (int m = 0; m < 4; ++m) \
      acc[m][(j0) + j] = MFMA(bfj, af[ks][m], acc[m][(j0) + j]); } \
    __builtin_amdgcn_s_setprio(0); SCHED; }
#define G1_TOGGLE() { aAddr0 ^= G1_ASLOT; aAddr1 ^= G1_ASLOT; bA0 += bdelta; bA1 += bdelta; bQ0 += bdelta; bQ1 += bdelta; bdelta = -bdelta; }

  if (grp == 0) {
    G1_LDA(0, 0); G1_LDA(0, 1); G1_LDA(0, 2); G1_LDA(0, 3);
    G1_LDB0(0); G1_LDB1(0); G1_LDB2(0); G1_LDB3(0); G1_LDB4(0);
    G1_LDA(1, 0); G1_LDA(1, 1); G1_LDA(1, 2); G1_LDA(1, 3);
    G1_LDB0(2); G1_LDB1(2); G1_LDB2(2);
    WAIT_V(7); BAR; SCHED;
#define G1_ITER0(i, LB0, LA, LB3, WEND) { \
      G1_RDB_LO(0); G1_RDA(); if (LB0) { G1_LDB3(2 * (i) + 2); G1_LDB4(2 * (i) + 2); } SCHED; BAR; G1_MMA(0, 0); BAR; SCHED; \
      G1_RDB_HI(0); SCHED; BAR; G1_MMA(0, 5); BAR; SCHED; \
      G1_RDB_LO(1); if (LA) { G1_LDA((i) + 2, 0); G1_LDA((i) + 2, 1); G1_LDA((i) + 2, 2); G1_LDA((i) + 2, 3); } SCHED; BAR; G1_MMA(1, 0); BAR; SCHED; \
      G1_RDB_HI(1); if (LB3) { G1_LDB0(2 * (i) + 4); G1_LDB1(2 * (i) + 4); G1_LDB2(2 * (i) + 4); } SCHED; BAR; G1_MMA(1, 5); WEND; BAR; SCHED; \
      G1_TOGGLE(); }
    for (int i = 0; i < 14; ++i) G1_ITER0(i, true, true, true, WAIT_V(7));
    G1_ITER0(14, true, false, false, WAIT_V(0));
    G1_ITER0(15, false, false, false, (void)0);
    BAR;
  } else {
    G1_LDA(0, 0); G1_LDA(0, 1); G1_LDA(0, 2); G1_LDA(0, 3);
    G1_LDB0(1); G1_LDB1(1); G1_LDB2(1); G1_LDB3(1); G1_LDB4(1);
    G1_LDA(1, 0); G1_LDA(1, 1); G1_LDA(1, 2); G1_LDA(1, 3);
    WAIT_V(9); BAR; SCHED;
    BAR; SCHED;
#define G1_ITER1(i, LB, LA, W1, W3) { \
      G1_RDB_LO(0); G1_RDA(); if (LB) { G1_LDB0(2 * (i) + 3); G1_LDB1(2 * (i) + 3); } SCHED; BAR; G1_MMA(0, 0); BAR; SCHED; \
      G1_RDB_HI(0); if (LB) { G1_LDB2(2 * (i) + 3); G1_LDB3(2 * (i) + 3); G1_LDB4(2 * (i) + 3); } SCHED; W1; BAR; G1_MMA(0, 5); BAR; SCHED; \
      G1_RDB_LO(1); if (LA) { G1_LDA((i) + 2, 0); G1_LDA((i) + 2, 1); } SCHED; BAR; G1_MMA(1, 0); BAR; SCHED; \
      G1_RDB_HI(1); if (LA) { G1_LDA((i) + 2, 2); G1_LDA((i) + 2, 3); } SCHED; W3; BAR; G1_MMA(1, 5); BAR; SCHED; \
      G1_TOGGLE(); }
    for (int i = 0; i < 14; ++i) G1_ITER1(i, true, true, WAIT_V(9), WAIT_V(9));
    G1_ITER1(14, true, false, WAIT_V(9), WAIT_V(5));
    G1_ITER1(15, false, false, WAIT_V(0), (void)0);
  }
  SCHED;

  int tz = threadIdx.x; asm volatile("" : "+v"(tz));
  const int ln = tz & 63, tok = ln & 15, qq = ln >> 4;
  const int row0 = rb * 256 + mw * 64;
  __bf16* Abuf = (__bf16*)(ws + WS_ABUF);
  const int grow = b * 8 + wave;
  const u32x4 gx0 = *(const u32x4*)(x + (size_t)grow * D_ + ln * 8);
  const u32x4 gx1 = *(const u32x4*)(x + (size_t)grow * D_ + 512 + ln * 8);
  const u32x4 gw0 = *(const u32x4*)(gate_w + ln * 8);
  const u32x4 gw1 = *(const u32x4*)(gate_w + 512 + ln * 8);

#define G1_HSTORE(jg, ju, fcol) _Pragma("unroll") for (int m = 0; m < 4; ++m) { \
      float h0 = bfr(acc[m][jg][0]) * bfr(acc[m][ju][0]), h1 = bfr(acc[m][jg][1]) * bfr(acc[m][ju][1]); \
      float h2 = bfr(acc[m][jg][2]) * bfr(acc[m][ju][2]), h3 = bfr(acc[m][jg][3]) * bfr(acc[m][ju][3]); \
      u32x2 v = {pack2(h0, h1), pack2(h2, h3)}; \
      *(u32x2*)(Abuf + (size_t)(row0 + m * 16 + tok) * KA_ + (fcol) + qq * 4) = v; }

  if (isA) {
    const int head = 2 * ct + grp;
    __bf16* pq = (__bf16*)(ws + WS_PQ);
    const unsigned kvb = lds0 + (unsigned)(grp * 256 * 136 + (mw * 64 + tok) * 136 + qq * 8);
#pragma unroll
    for (int m = 0; m < 4; ++m) {
#pragma unroll
      for (int jt = 0; jt < 2; ++jt) {
        u32x2 vk = {pack2(phi_f(acc[m][2 + jt][0]), phi_f(acc[m][2 + jt][1])), pack2(phi_f(acc[m][2 + jt][2]), phi_f(acc[m][2 + jt][3]))};
        *(__attribute__((address_space(3))) u32x2*)(size_t)(kvb + m * 16 * 136 + jt * 32) = vk;
        u32x2 vv = {pack2(acc[m][4 + jt][0], acc[m][4 + jt][1]), pack2(acc[m][4 + jt][2], acc[m][4 + jt][3])};
        *(__attribute__((address_space(3))) u32x2*)(size_t)(kvb + m * 16 * 136 + 64 + jt * 32) = vv;
        u32x2 vq = {pack2(phi_f(acc[m][jt][0]), phi_f(acc[m][jt][1])), pack2(phi_f(acc[m][jt][2]), phi_f(acc[m][jt][3]))};
        *(u32x2*)(pq + (size_t)(row0 + m * 16 + tok) * D_ + head * 32 + jt * 16 + qq * 4) = vq;
      }
    }
    G1_HSTORE(6, 8, f0 + grp * 32);
    G1_HSTORE(7, 9, f0 + grp * 32 + 16);
    __syncthreads();
    const int dt = (wave >> 1) & 1, et = wave & 1;
    const int tq = (ln & 15) >> 2, tp = ln & 3;
    const unsigned rbase = lds0 + (unsigned)(grp * 256 * 136 + (8 * qq + tq) * 136 + tp * 8);
    f32x4 kv = {0.f, 0.f, 0.f, 0.f}, zz = {0.f, 0.f, 0.f, 0.f};
    const short one = 0x3F80;
    const s16x8 ones = {one, one, one, one, one, one, one, one};
#pragma unroll
    for (int s = 0; s < 8; ++s) {
      s16x4 k0 = lds_tr(rbase + s * 32 * 136 + dt * 32), k1 = lds_tr(rbase + s * 32 * 136 + 4 * 136 + dt * 32);
      s16x4 v0 = lds_tr(rbase + s * 32 * 136 + 64 + et * 32), v1 = lds_tr(rbase + s * 32 * 136 + 4 * 136 + 64 + et * 32);
      s16x8 kf = __builtin_shufflevector(k0, k1, 0, 1, 2, 3, 4, 5, 6, 7);
      s16x8 vf = __builtin_shufflevector(v0, v1, 0, 1, 2, 3, 4, 5, 6, 7);
      kv = MFMA(kf, vf, kv);
      zz = MFMA(kf, ones, zz);
    }
    float* kvp = (float*)(ws + WS_KVP) + ((size_t)rb * 32 + head) * 1024;
    *(f32x4*)(kvp + (et * 16 + tok) * 32 + dt * 16 + qq * 4) = kv;
    if (et == 0 && tok == 0) *(f32x4*)((float*)(ws + WS_ZP) + ((size_t)rb * 32 + head) * 32 + dt * 16 + qq * 4) = zz;
  } else {
    G1_HSTORE(0, 2, f0 + (2 * grp) * 16);
    G1_HSTORE(1, 3, f0 + (2 * grp + 1) * 16);
    G1_HSTORE(4, 6, f0 + (4 + 2 * grp) * 16);
    G1_HSTORE(5, 7, f0 + (5 + 2 * grp) * 16);
    G1_HSTORE(8, 9, f0 + (8 + grp) * 16);
  }
  {
    float s = 0.f;
#pragma unroll
    for (int i = 0; i < 4; ++i) {
      unsigned xa0 = gx0[i], wa0 = gw0[i], xa1 = gx1[i], wa1 = gw1[i];
      s += bfr(__builtin_bit_cast(float, xa0 << 16) * __builtin_bit_cast(float, wa0 << 16));
      s += bfr(__builtin_bit_cast(float, xa0 & 0xffff0000u) * __builtin_bit_cast(float, wa0 & 0xffff0000u));
      s += bfr(__builtin_bit_cast(float, xa1 << 16) * __builtin_bit_cast(float, wa1 << 16));
      s += bfr(__builtin_bit_cast(float, xa1 & 0xffff0000u) * __builtin_bit_cast(float, wa1 & 0xffff0000u));
    }
#pragma unroll
    for (int o = 32; o >= 1; o >>= 1) s += __shfl_xor(s, o);
    if (ln == 0) {
      float t = bfr(s);
      ((float*)(ws + WS_G))[grow] = bfr(1.0f / (1.0f + __expf(-t)));
    }
    if (b == 0 && tz < 8) *(unsigned*)(ws + WS_CNT + tz * 256) = 0u;
  }
}

#define G2_NS 6
#define G2_STAGE 24576
#define G2_FLAG (G2_NS * G2_STAGE)
#define G2_LDS (G2_FLAG + 64)
#define G2_NH 4

__global__ __launch_bounds__(768, 3) void gemm2_kernel(const __bf16* __restrict__ wo, const __bf16* __restrict__ wd,
                                                       char* ws, __bf16* __restrict__ out) {
  extern __shared__ __attribute__((aligned(16))) char smem[];
  const unsigned lds0 = (unsigned)(size_t)(lds_char_p)smem;
  volatile __attribute__((address_space(3))) unsigned* lflag = (volatile __attribute__((address_space(3))) unsigned*)(size_t)(lds0 + G2_FLAG);
  const int b = blockIdx.x;
  const int xcd = b & 7, idx = b >> 3;
  const int rt = (xcd >> 1) * 4 + (idx & 3), ctile = (xcd & 1) * 8 + (idx >> 2);
  const int wave = __builtin_amdgcn_readfirstlane(threadIdx.x >> 6);
  const int l = threadIdx.x & 63;
  __bf16* Abuf = (__bf16*)(ws + WS_ABUF);
  if (threadIdx.x == 0) *lflag = 0u;
  __syncthreads();

  if (wave >= 8) {
    const int hw = wave - 8, head = b & 31, hrb = b >> 5;
    const int e = l & 15, q = l >> 4;
    const float* kvp = (const float*)(ws + WS_KVP) + (size_t)head * 1024;
    const float* zp = (const float*)(ws + WS_ZP) + (size_t)head * 32;
    const __bf16* pq = (const __bf16*)(ws + WS_PQ);
    const int r0 = hrb * 256 + hw * 64;
    s16x8 pf[4];
#pragma unroll
    for (int m = 0; m < 4; ++m) pf[m] = *(const s16x8*)(pq + (size_t)(r0 + m * 16 + e) * D_ + head * 32 + q * 8);
    f32x4 s0a = {0, 0, 0, 0}, s0b = s0a, s1a = s0a, s1b = s0a, za = s0a, zb = s0a;
#pragma unroll
    for (int r = 0; r < 8; ++r) {
      const float* p = kvp + (size_t)r * 32 * 1024;
      s0a += *(const f32x4*)(p + e * 32 + q * 8);
      s0b += *(const f32x4*)(p + e * 32 + q * 8 + 4);
      s1a += *(const f32x4*)(p + (16 + e) * 32 + q * 8);
      s1b += *(const f32x4*)(p + (16 + e) * 32 + q * 8 + 4);
      za += *(const f32x4*)(zp + (size_t)r * 32 * 32 + q * 8);
      zb += *(const f32x4*)(zp + (size_t)r * 32 * 32 + q * 8 + 4);
    }
    u32x4 k0 = {pack2(s0a[0], s0a[1]), pack2(s0a[2], s0a[3]), pack2(s0b[0], s0b[1]), pack2(s0b[2], s0b[3])};
    u32x4 k1 = {pack2(s1a[0], s1a[1]), pack2(s1a[2], s1a[3]), pack2(s1b[0], s1b[1]), pack2(s1b[2], s1b[3])};
    u32x4 kz = {pack2(za[0], za[1]), pack2(za[2], za[3]), pack2(zb[0], zb[1]), pack2(zb[2], zb[3])};
    const s16x8 kf0 = __builtin_bit_cast(s16x8, k0), kf1 = __builtin_bit_cast(s16x8, k1), zf = __builtin_bit_cast(s16x8, kz);
#pragma unroll
    for (int m = 0; m < 4; ++m) {
      f32x4 z4 = {0, 0, 0, 0};
      f32x4 n0 = MFMA(kf0, pf[m], z4), n1 = MFMA(kf1, pf[m], z4), dn = MFMA(zf, pf[m], z4);
      const float den = bfr(bfr(dn[0]) + 1e-6f);
      unsigned long long o0 = (unsigned long long)pack2(bfr(n0[0]) / den, bfr(n0[1]) / den) | ((unsigned long long)pack2(bfr(n0[2]) / den, bfr(n0[3]) / den) << 32);
      unsigned long long o1 = (unsigned long long)pack2(bfr(n1[0]) / den, bfr(n1[1]) / den) | ((unsigned long long)pack2(bfr(n1[2]) / den, bfr(n1[3]) / den) << 32);
      __bf16* dst = Abuf + (size_t)(r0 + m * 16 + e) * KA_ + F_ + head * 32 + q * 4;
      __hip_atomic_store((unsigned long long*)dst, o0, __ATOMIC_RELAXED, __HIP_MEMORY_SCOPE_AGENT);
      __hip_atomic_store((unsigned long long*)(dst + 16), o1, __ATOMIC_RELAXED, __HIP_MEMORY_SCOPE_AGENT);
    }
    WAIT_V(0);
    if (l == 0) __hip_atomic_fetch_add((unsigned*)(ws + WS_CNT + hrb * 256), 1u, __ATOMIC_RELAXED, __HIP_MEMORY_SCOPE_AGENT);
    if (hw == 0) {
      unsigned* cnt = (unsigned*)(ws + WS_CNT + (rt >> 1) * 256);
      while (__hip_atomic_load(cnt, __ATOMIC_RELAXED, __HIP_MEMORY_SCOPE_AGENT) < 32u * G2_NH) __builtin_amdgcn_s_sleep(8);
      __builtin_amdgcn_fence(__ATOMIC_ACQUIRE, "agent");
      WAIT_V(0);
      if (l == 0) *lflag = 1u;
    }
    return;
  }

  const int kg = wave >> 2, wm = (wave >> 1) & 1, wn = wave & 1;
  const char* asrc = (const char*)(Abuf + (size_t)(rt * 128 + wave * 16) * KA_);
  const unsigned voffA = (unsigned)((l >> 3) * (KA_ * 2) + (((l & 7) ^ (l >> 3)) * 16));
  const int krow = 8 * wave + (l >> 3);
  const int cl = (l & 7) ^ ((((l >> 4) & 1) | ((wave & 1) << 1)) << 1);
  const unsigned voffB = (unsigned)(krow * 2048 + cl * 16);
  const char* bsrc_d = (const char*)(wd + ctile * 64);
  const char* bsrc_o = (const char*)(wo + ctile * 64);
#define G2_LOAD(s, slot) { const unsigned _d = lds0 + (unsigned)(slot) * G2_STAGE; \
    glds16(asrc + (size_t)(s) * 128 + voffA, _d + wave * 2048); \
    glds16(asrc + (size_t)(s) * 128 + 8 * KA_ * 2 + voffA, _d + wave * 2048 + 1024); \
    glds16(((s) < 56 ? bsrc_d + (size_t)(s) * 131072 : bsrc_o + (size_t)((s) - 56) * 131072) + voffB, _d + 16384 + wave * 1024); }

  const int g4 = l >> 4, q4 = (l & 15) >> 2, pp = l & 3;
  const unsigned aOff = (unsigned)((wm * 64 + (l & 15)) * 128 + (((kg * 4 + g4) ^ (l & 7)) * 16));
  const int sh = ((q4 >> 1) & 1) | ((g4 & 1) << 1);
  const unsigned bOffL = 16384u + (unsigned)((kg * 32 + 8 * g4 + q4) * 128 + (pp >> 1) * 16 + (pp & 1) * 8);
  const unsigned bOff0 = bOffL + (unsigned)(((2 * wn) ^ sh) * 32), bOff1 = bOffL + (unsigned)(((2 * wn + 1) ^ sh) * 32);

  f32x4 accb[4][2], acca[4][2];
#pragma unroll
  for (int m = 0; m < 4; ++m) { accb[m][0] = accb[m][1] = acca[m][0] = acca[m][1] = f32x4{0.f, 0.f, 0.f, 0.f}; }
  s16x8 afA[4], afB[4];
  s16x4 bfA[4], bfB[4];
#define G2_RD(af, bf, slot) { const unsigned _s = lds0 + (unsigned)(slot) * G2_STAGE; \
    const unsigned _a = _s + aOff, _b0 = _s + bOff0, _b1 = _s + bOff1; \
    ARD128(af[0], _a, 0); ARD128(af[1], _a, 2048); ARD128(af[2], _a, 4096); ARD128(af[3], _a, 6144); \
    ATRF(bf[0], bf[1], _b0, 0); ATRF(bf[2], bf[3], _b1, 0); }
#define G2_MMA(acc, af, bf) { const s16x8 _f0 = CAT(bf[0], bf[1]), _f1 = CAT(bf[2], bf[3]); \
    _Pragma("unroll") for (int m = 0; m < 4; ++m) { acc[m][0] = MFMA(_f0, af[m], acc[m][0]); acc[m][1] = MFMA(_f1, af[m], acc[m][1]); } }

#pragma unroll
  for (int s = 0; s < G2_NS; ++s) G2_LOAD(s, s);
  WAIT_V(15); BAR; SCHED;
  G2_RD(afA, bfA, 0);
  int slot = 0;
#define G2_STEP(s, acc, afc, bfc, afn, bfn, DOLOAD, VM) { \
    WAIT_L(0); VM; BAR; SCHED; \
    if (DOLOAD) G2_LOAD((s) + G2_NS, slot); \
    slot = slot == G2_NS - 1 ? 0 : slot + 1; \
    G2_RD(afn, bfn, slot); SCHED; \
    G2_MMA(acc, afc, bfc); SCHED; }
  for (int s = 0; s < 50; s += 2) {
    G2_STEP(s, accb, afA, bfA, afB, bfB, true, WAIT_V(12));
    G2_STEP(s + 1, accb, afB, bfB, afA, bfA, true, WAIT_V(12));
  }
  while (*lflag == 0u) __builtin_amdgcn_s_sleep(2);
  SCHED;
  for (int s = 50; s < 56; s += 2) {
    G2_STEP(s, accb, afA, bfA, afB, bfB, true, WAIT_V(12));
    G2_STEP(s + 1, accb, afB, bfB, afA, bfA, true, WAIT_V(12));
  }
  for (int s = 56; s < 66; s += 2) {
    G2_STEP(s, acca, afA, bfA, afB, bfB, true, WAIT_V(12));
    G2_STEP(s + 1, acca, afB, bfB, afA, bfA, true, WAIT_V(12));
  }
  G2_STEP(66, acca, afA, bfA, afB, bfB, false, WAIT_V(12));
  G2_STEP(67, acca, afB, bfB, afA, bfA, false, WAIT_V(9));
  G2_STEP(68, acca, afA, bfA, afB, bfB, false, WAIT_V(6));
  G2_STEP(69, acca, afB, bfB, afA, bfA, false, WAIT_V(3));
  G2_STEP(70, acca, afA, bfA, afB, bfB, false, WAIT_V(0));
  WAIT_L(0); SCHED;
  G2_MMA(acca, afB, bfB);
  BAR; SCHED;
  const unsigned red = lds0 + (unsigned)((wave & 3) * 16384 + l * 16);
  if (kg == 1) {
#pragma unroll
    for (int m = 0; m < 4; ++m)
#pragma unroll
      for (int n = 0; n < 2; ++n) {
        *(__attribute__((address_space(3))) f32x4*)(size_t)(red + (m * 2 + n) * 1024) = acca[m][n];
        *(__attribute__((address_space(3))) f32x4*)(size_t)(red + 8192 + (m * 2 + n) * 1024) = accb[m][n];
      }
  }
  WAIT_L(0); BAR; SCHED;
  if (kg == 0) {
    const float* gv = (const float*)(ws + WS_G);
#pragma unroll
    for (int m = 0; m < 4; ++m) {
      const int row = rt * 128 + wm * 64 + m * 16 + (l & 15);
      const float g = gv[row], og = bfr(1.0f - g);
#pragma unroll
      for (int n = 0; n < 2; ++n) {
        f32x4 a = acca[m][n] + *(__attribute__((address_space(3))) f32x4*)(size_t)(red + (m * 2 + n) * 1024);
        f32x4 bb = accb[m][n] + *(__attribute__((address_space(3))) f32x4*)(size_t)(red + 8192 + (m * 2 + n) * 1024);
        float o[4];
#pragma unroll
        for (int r = 0; r < 4; ++r) o[r] = bfr(g * bfr(a[r])) + bfr(og * bfr(bb[r]));
        u32x2 v = {pack2(o[0], o[1]), pack2(o[2], o[3])};
        *(u32x2*)(out + (size_t)row * D_ + ctile * 64 + wn * 32 + n * 16 + g4 * 4) = v;
      }
    }
  }
}

extern "C" void kernel_launch(void* const* d_in, const int* in_sizes, int n_in, void* d_out, int out_size, void* d_ws,
                              size_t ws_size, hipStream_t stream) {
  const __bf16* x = (const __bf16*)d_in[0];
  const __bf16* wqkv = (const __bf16*)d_in[1];
  const __bf16* wo = (const __bf16*)d_in[2];
  const __bf16* wg = (const __bf16*)d_in[4];
  const __bf16* wu = (const __bf16*)d_in[5];
  const __bf16* wd = (const __bf16*)d_in[6];
  const __bf16* gw = (const __bf16*)d_in[7];
  hipFuncSetAttribute((const void*)gemm1_kernel, hipFuncAttributeMaxDynamicSharedMemorySize, G1_LDS);
  hipFuncSetAttribute((const void*)gemm2_kernel, hipFuncAttributeMaxDynamicSharedMemorySize, G2_LDS);
  gemm1_kernel<<<256, 512, G1_LDS, stream>>>(x, wqkv, wg, wu, gw, (char*)d_ws);
  gemm2_kernel<<<256, 768, G2_LDS, stream>>>(wo, wd, (char*)d_ws, (__bf16*)d_out);
}
```

```cpp
#include <hip/hip_runtime.h>
#include <hip/hip_bf16.h>
#include <stdint.h>

typedef __attribute__((ext_vector_type(8))) short s16x8;
typedef __attribute__((ext_vector_type(4))) short s16x4;
typedef __attribute__((ext_vector_type(4))) float f32x4;
typedef __attribute__((ext_vector_type(2))) float f32x2;
typedef __attribute__((ext_vector_type(2))) __bf16 bf16x2;
typedef __attribute__((ext_vector_type(2))) unsigned u32x2;
typedef __attribute__((ext_vector_type(4))) unsigned u32x4;
typedef __attribute__((address_space(3))) s16x4* lds_s16x4_p;
typedef __attribute__((address_space(3))) s16x8* lds_s16x8_p;
typedef __attribute__((address_space(3))) char* lds_char_p;

#define S_ 2048
#define D_ 1024
#define F_ 3584
#define AIDX(row, col) ((size_t)(((row) >> 7) * 72 + ((col) >> 6)) * 8192 + (size_t)(((row) & 127) * 64 + ((col) & 63)))
#define PQIDX(row, head, d) ((size_t)(head) * 65536 + (size_t)((row) * 32 + (d)))
#define KA_ 4608

#define WS_ABUF 0ull
#define WS_PQ   (WS_ABUF + (size_t)S_ * KA_ * 2)
#define WS_KVP  (WS_PQ + (size_t)S_ * D_ * 2)
#define WS_ZP   (WS_KVP + (size_t)8 * 32 * 1024 * 4)
#define WS_G    (WS_ZP + (size_t)8 * 32 * 32 * 4)
#define WS_CNT  (WS_G + (size_t)S_ * 4)
#define WS_PART (WS_CNT + 16384)

#define STAMPK(kid, i) do {} while (0)
#define WAIT_V(n) asm volatile("s_waitcnt vmcnt(" #n ")" ::: "memory")
#define WAIT_L(n) asm volatile("s_waitcnt lgkmcnt(" #n ")" ::: "memory")
#define BAR __builtin_amdgcn_s_barrier()
#define SCHED __builtin_amdgcn_sched_barrier(0)

__device__ __forceinline__ float bfr(float v) { return (float)(__bf16)v; }
__device__ __forceinline__ unsigned pack2(float a, float b) {
  f32x2 v = {a, b};
  return __builtin_bit_cast(unsigned, __builtin_convertvector(v, bf16x2));
}
__device__ __forceinline__ float phi_f(float acc) {
  float t = bfr(acc);
  float r = t > 0.f ? bfr(t + 1.0f) : bfr(__expf(t));
  return bfr(r + 1e-6f);
}
__device__ __forceinline__ s16x8 lds_rd128(unsigned addr) { return *(lds_s16x8_p)(size_t)addr; }
__device__ __forceinline__ s16x4 lds_tr(unsigned addr) {
  return __builtin_amdgcn_ds_read_tr16_b64_v4i16((lds_s16x4_p)(size_t)addr);
}
__device__ __forceinline__ s16x8 lds_trfrag(unsigned addr) {
  s16x4 lo = lds_tr(addr), hi = lds_tr(addr + 512);
  return __builtin_shufflevector(lo, hi, 0, 1, 2, 3, 4, 5, 6, 7);
}
#define ARD128(dst, addr, off) asm volatile("ds_read_b128 %0, %1 offset:%2" : "=v"(dst) : "v"(addr), "i"(off) : "memory")
#define ATR(dst, addr, off) asm volatile("ds_read_b64_tr_b16 %0, %1 offset:%2" : "=v"(dst) : "v"(addr), "i"(off) : "memory")
#define ATRF(lo, hi, addr, off) { ATR(lo, addr, off); ATR(hi, addr, (off) + 512); }
#define CAT(lo, hi) __builtin_shufflevector(lo, hi, 0, 1, 2, 3, 4, 5, 6, 7)
__device__ __forceinline__ void glds16(const void* g, unsigned lds_addr) {
  __builtin_amdgcn_global_load_lds((const __attribute__((address_space(1))) unsigned*)g,
                                   (__attribute__((address_space(3))) unsigned*)(size_t)lds_addr, 16, 0, 0);
}
#define MFMA(a, b, c) __builtin_amdgcn_mfma_f32_16x16x32_bf16(a, b, c, 0, 0, 0)

#define G1_ASLOT 32768
#define G1_BBASE 65536
#define G1_BSLOT 20480
#define G1_PFB (G1_BBASE + 4 * G1_BSLOT)
#define G1_LDS (G1_PFB + 1024)
#define G1_PFW0 3

__global__ __launch_bounds__(512, 2) void gemm1_kernel(const __bf16* __restrict__ x, const __bf16* __restrict__ wqkv,
                                                       const __bf16* __restrict__ wg, const __bf16* __restrict__ wu,
                                                       const __bf16* __restrict__ gate_w, char* __restrict__ ws,
                                                       const __bf16* __restrict__ wd, const __bf16* __restrict__ wo) {
  extern __shared__ __attribute__((aligned(16))) char smem[];
  const unsigned lds0 = (unsigned)(size_t)(lds_char_p)smem;
  const int b = blockIdx.x;
  const int xcd = b & 7, idx = b >> 3, rb = idx & 7, ct = xcd * 4 + (idx >> 3);
  const bool isA = ct < 16;
  const int f0 = isA ? ct * 64 : 1024 + (ct - 16) * 160;
  const int wave = __builtin_amdgcn_readfirstlane(threadIdx.x >> 6);
  const int l = threadIdx.x & 63;
  const int grp = wave >> 2, mw = wave & 3;
  STAMPK(0, 0);

  const char* xa = (const char*)x + (size_t)(rb * 256 + wave * 32) * 2048;
  const unsigned voffA = (unsigned)((l >> 3) * 2048 + (((l & 7) ^ (l >> 3)) * 16));
  const int krow = 8 * mw + (l >> 3);
  const int cl = (l & 7) ^ ((((l >> 4) & 1) | ((mw & 1) << 1)) << 1);
  const int rs0 = isA ? 3072 : F_;
  const unsigned voffQ = (unsigned)(krow * rs0 * 2 + cl * 16);
  const unsigned voffF = (unsigned)(krow * F_ * 2 + cl * 16);
  const char* sb0 = isA ? (const char*)(wqkv + ct * 64) : (const char*)(wg + f0);
  const char* sb1 = isA ? (const char*)(wqkv + 1024 + ct * 64) : (const char*)(wu + f0);
  const char* sb2 = isA ? (const char*)(wqkv + 2048 + ct * 64) : (const char*)(wg + f0 + 64);
  const char* sb3 = isA ? (const char*)(wg + f0) : (const char*)(wu + f0 + 64);
  const char* p4 = isA ? (const char*)(wu + f0 + cl * 8)
                       : (cl < 4 ? (const char*)(wg + f0 + 128 + cl * 8) : (const char*)(wu + f0 + 128 + (cl - 4) * 8));
  p4 += (size_t)krow * F_ * 2;
  const size_t pst0 = (size_t)32 * rs0 * 2, pstF = (size_t)32 * F_ * 2;

#define G1_LDA(kt, i) glds16(xa + (size_t)(kt) * 128 + (i) * 16384 + voffA, lds0 + ((kt) & 1) * G1_ASLOT + wave * 4096 + (i) * 1024)
#define G1_BDST(p, s) (lds0 + G1_BBASE + ((p) & 3) * G1_BSLOT + (s) * 4096 + mw * 1024)
#define G1_LDB0(p) glds16(sb0 + (size_t)(p) * pst0 + voffQ, G1_BDST(p, 0))
#define G1_LDB1(p) glds16(sb1 + (size_t)(p) * pst0 + voffQ, G1_BDST(p, 1))
#define G1_LDB2(p) glds16(sb2 + (size_t)(p) * pst0 + voffQ, G1_BDST(p, 2))
#define G1_LDB3(p) glds16(sb3 + (size_t)(p) * pstF + voffF, G1_BDST(p, 3))
#define G1_LDB4(p) glds16(p4 + (size_t)(p) * pstF, G1_BDST(p, 4))

#define G1_PF(sl) { int _l = threadIdx.x & 63; asm volatile("" : "+v"(_l)); \
    const int _s = (sl) + mw, _c = _s > 8 ? 8 : _s; \
    const char* _p = (_c < 7 ? (const char*)wd + (size_t)_c * 1048576 : (const char*)wo + (size_t)(_c - 7) * 1048576) + (size_t)(unsigned)((b * 64 + _l) * 64); \
    __builtin_amdgcn_global_load_lds((const __attribute__((address_space(1))) unsigned*)_p, \
                                     (__attribute__((address_space(3))) unsigned*)(size_t)(lds0 + G1_PFB + mw * 256), 4, 0, 0); }

  const int g4 = l >> 4, q4 = (l & 15) >> 2, pp = l & 3;
  unsigned aAddr0 = lds0 + (unsigned)((mw * 64 + (l & 15)) * 128 + ((g4 ^ (l & 7)) * 16));
  unsigned aAddr1 = aAddr0 ^ 64u;
  const int sh = ((q4 >> 1) & 1) | ((g4 & 1) << 1);
  const unsigned bl = lds0 + G1_BBASE + (unsigned)((8 * g4 + q4) * 128 + (pp >> 1) * 16 + (pp & 1) * 8);
  unsigned bA0 = bl + (unsigned)(((2 * grp) ^ sh) * 32);
  unsigned bA1 = bl + (unsigned)(((2 * grp + 1) ^ sh) * 32);
  unsigned bQ0 = bl + 4 * 4096 + (unsigned)(((isA ? 2 * grp : grp) ^ sh) * 32);
  unsigned bQ1 = bl + 4 * 4096 + (unsigned)(((isA ? 2 * grp + 1 : grp + 2) ^ sh) * 32);
  int bdelta = 2 * G1_BSLOT;

  f32x4 acc[4][10];
#pragma unroll
  for (int m = 0; m < 4; ++m)
#pragma unroll
    for (int j = 0; j < 10; ++j) acc[m][j] = f32x4{0.f, 0.f, 0.f, 0.f};
  s16x8 af[2][4];
  s16x4 bl_[5], bh_[5];

#define G1_RDA() { ARD128(af[0][0], aAddr0, 0); ARD128(af[0][1], aAddr0, 2048); ARD128(af[0][2], aAddr0, 4096); ARD128(af[0][3], aAddr0, 6144); \
    ARD128(af[1][0], aAddr1, 0); ARD128(af[1][1], aAddr1, 2048); ARD128(af[1][2], aAddr1, 4096); ARD128(af[1][3], aAddr1, 6144); }
#define G1_RDB_LO(ps) { ATRF(bl_[0], bh_[0], bA0, (ps) * G1_BSLOT); ATRF(bl_[1], bh_[1], bA1, (ps) * G1_BSLOT); \
    ATRF(bl_[2], bh_[2], bA0, (ps) * G1_BSLOT + 4096); ATRF(bl_[3], bh_[3], bA1, (ps) * G1_BSLOT + 4096); \
    ATRF(bl_[4], bh_[4], bA0, (ps) * G1_BSLOT + 8192); }
#define G1_RDB_HI(ps) { ATRF(bl_[0], bh_[0], bA1, (ps) * G1_BSLOT + 8192); ATRF(bl_[1], bh_[1], bA0, (ps) * G1_BSLOT + 12288); \
    ATRF(bl_[2], bh_[2], bA1, (ps) * G1_BSLOT + 12288); ATRF(bl_[3], bh_[3], bQ0, (ps) * G1_BSLOT); \
    ATRF(bl_[4], bh_[4], bQ1, (ps) * G1_BSLOT); }
#define G1_MMA(ks, j0) { WAIT_L(0); SCHED; __builtin_amdgcn_s_setprio(1); \
    _Pragma("unroll") for (int j = 0; j < 5; ++j) { const s16x8 bfj = CAT(bl_[j], bh_[j]); _Pragma("unroll") for (int m = 0; m < 4; ++m) \
      acc[m][(j0) + j] = MFMA(bfj, af[ks][m], acc[m][(j0) + j]); } \
    __builtin_amdgcn_s_setprio(0); SCHED; }
#define G1_TOGGLE() { aAddr0 ^= G1_ASLOT; aAddr1 ^= G1_ASLOT; bA0 += bdelta; bA1 += bdelta; bQ0 += bdelta; bQ1 += bdelta; bdelta = -bdelta; }

  if (grp == 0) {
    G1_LDA(0, 0); G1_LDA(0, 1); G1_LDA(0, 2); G1_LDA(0, 3);
    G1_LDB0(0); G1_LDB1(0); G1_LDB2(0); G1_LDB3(0); G1_LDB4(0);
    G1_LDA(1, 0); G1_LDA(1, 1); G1_LDA(1, 2); G1_LDA(1, 3);
    G1_LDB0(2); G1_LDB1(2); G1_LDB2(2);
    WAIT_V(7); BAR; SCHED;
    STAMPK(0, 1);
#define G1_ITER0(i, LB0, LA, LB3, WEND) { \
      G1_RDB_LO(0); G1_RDA(); if (LB0) { G1_LDB3(2 * (i) + 2); G1_LDB4(2 * (i) + 2); } SCHED; BAR; G1_MMA(0, 0); BAR; SCHED; \
      if (LA && (unsigned)((i) - G1_PFW0) < 3u) { G1_PF(4 * ((i) - G1_PFW0)); SCHED; } G1_RDB_HI(0); SCHED; BAR; G1_MMA(0, 5); BAR; SCHED; \
      G1_RDB_LO(1); if (LA) { G1_LDA((i) + 2, 0); G1_LDA((i) + 2, 1); G1_LDA((i) + 2, 2); G1_LDA((i) + 2, 3); } SCHED; BAR; G1_MMA(1, 0); BAR; SCHED; \
      G1_RDB_HI(1); if (LB3) { G1_LDB0(2 * (i) + 4); G1_LDB1(2 * (i) + 4); G1_LDB2(2 * (i) + 4); } SCHED; BAR; G1_MMA(1, 5); WEND; BAR; SCHED; \
      G1_TOGGLE(); }
    for (int i = 0; i < 14; ++i) G1_ITER0(i, true, true, true, if ((unsigned)(i - G1_PFW0) < 3u) WAIT_V(8); else WAIT_V(7));
    G1_ITER0(14, true, false, false, WAIT_V(0));
    G1_ITER0(15, false, false, false, (void)0);
    BAR;
  } else {
    G1_LDA(0, 0); G1_LDA(0, 1); G1_LDA(0, 2); G1_LDA(0, 3);
    G1_LDB0(1); G1_LDB1(1); G1_LDB2(1); G1_LDB3(1); G1_LDB4(1);
    G1_LDA(1, 0); G1_LDA(1, 1); G1_LDA(1, 2); G1_LDA(1, 3);
    WAIT_V(9); BAR; SCHED;
    BAR; SCHED;
#define G1_ITER1(i, LB, LA, W1, W3) { \
      G1_RDB_LO(0); G1_RDA(); if (LB) { G1_LDB0(2 * (i) + 3); G1_LDB1(2 * (i) + 3); } SCHED; BAR; G1_MMA(0, 0); BAR; SCHED; \
      G1_RDB_HI(0); if (LB) { G1_LDB2(2 * (i) + 3); G1_LDB3(2 * (i) + 3); G1_LDB4(2 * (i) + 3); } SCHED; W1; BAR; G1_MMA(0, 5); BAR; SCHED; \
      G1_RDB_LO(1); if (LA) { G1_LDA((i) + 2, 0); G1_LDA((i) + 2, 1); } SCHED; BAR; G1_MMA(1, 0); BAR; SCHED; \
      G1_RDB_HI(1); if (LA) { G1_LDA((i) + 2, 2); G1_LDA((i) + 2, 3); } SCHED; W3; BAR; G1_MMA(1, 5); BAR; SCHED; \
      G1_TOGGLE(); }
    for (int i = 0; i < 14; ++i) G1_ITER1(i, true, true, WAIT_V(9), WAIT_V(9));
    G1_ITER1(14, true, false, WAIT_V(9), WAIT_V(5));
    G1_ITER1(15, false, false, WAIT_V(0), (void)0);
  }
  SCHED;
  STAMPK(0, 2);

  int tz = threadIdx.x; asm volatile("" : "+v"(tz));
  const int ln = tz & 63, tok = ln & 15, qq = ln >> 4;
  const int row0 = rb * 256 + mw * 64;
  __bf16* Abuf = (__bf16*)(ws + WS_ABUF);
  const int grow = b * 8 + wave;
  const u32x4 gx0 = *(const u32x4*)(x + (size_t)grow * D_ + ln * 8);
  const u32x4 gx1 = *(const u32x4*)(x + (size_t)grow * D_ + 512 + ln * 8);
  const u32x4 gw0 = *(const u32x4*)(gate_w + ln * 8);
  const u32x4 gw1 = *(const u32x4*)(gate_w + 512 + ln * 8);

#define G1_HSTORE(jg, ju, fcol) _Pragma("unroll") for (int m = 0; m < 4; ++m) { \
      float h0 = bfr(acc[m][jg][0]) * bfr(acc[m][ju][0]), h1 = bfr(acc[m][jg][1]) * bfr(acc[m][ju][1]); \
      float h2 = bfr(acc[m][jg][2]) * bfr(acc[m][ju][2]), h3 = bfr(acc[m][jg][3]) * bfr(acc[m][ju][3]); \
      u32x2 v = {pack2(h0, h1), pack2(h2, h3)}; \
      *(u32x2*)(Abuf + AIDX(row0 + m * 16, fcol) + tok * 64 + qq * 4) = v; }

  if (isA) {
    const int head = 2 * ct + grp;
    __bf16* pq = (__bf16*)(ws + WS_PQ);
    const unsigned kvb = lds0 + (unsigned)(grp * 256 * 136 + (mw * 64 + tok) * 136 + qq * 8);
#pragma unroll
    for (int m = 0; m < 4; ++m) {
#pragma unroll
      for (int jt = 0; jt < 2; ++jt) {
        u32x2 vk = {pack2(phi_f(acc[m][2 + jt][0]), phi_f(acc[m][2 + jt][1])), pack2(phi_f(acc[m][2 + jt][2]), phi_f(acc[m][2 + jt][3]))};
        *(__attribute__((address_space(3))) u32x2*)(size_t)(kvb + m * 16 * 136 + jt * 32) = vk;
        u32x2 vv = {pack2(acc[m][4 + jt][0], acc[m][4 + jt][1]), pack2(acc[m][4 + jt][2], acc[m][4 + jt][3])};
        *(__attribute__((address_space(3))) u32x2*)(size_t)(kvb + m * 16 * 136 + 64 + jt * 32) = vv;
        u32x2 vq = {pack2(phi_f(acc[m][jt][0]), phi_f(acc[m][jt][1])), pack2(phi_f(acc[m][jt][2]), phi_f(acc[m][jt][3]))};
        *(u32x2*)(pq + PQIDX(row0 + m * 16 + tok, head, jt * 16 + qq * 4)) = vq;
      }
    }
    G1_HSTORE(6, 8, f0 + grp * 32);
    G1_HSTORE(7, 9, f0 + grp * 32 + 16);
    __syncthreads();
    const int dt = (wave >> 1) & 1, et = wave & 1;
    const int tq = (ln & 15) >> 2, tp = ln & 3;
    const unsigned rbase = lds0 + (unsigned)(grp * 256 * 136 + (8 * qq + tq) * 136 + tp * 8);
    f32x4 kv = {0.f, 0.f, 0.f, 0.f}, zz = {0.f, 0.f, 0.f, 0.f};
    const short one = 0x3F80;
    const s16x8 ones = {one, one, one, one, one, one, one, one};
#pragma unroll
    for (int s = 0; s < 8; ++s) {
      s16x4 k0 = lds_tr(rbase + s * 32 * 136 + dt * 32), k1 = lds_tr(rbase + s * 32 * 136 + 4 * 136 + dt * 32);
      s16x4 v0 = lds_tr(rbase + s * 32 * 136 + 64 + et * 32), v1 = lds_tr(rbase + s * 32 * 136 + 4 * 136 + 64 + et * 32);
      s16x8 kf = __builtin_shufflevector(k0, k1, 0, 1, 2, 3, 4, 5, 6, 7);
      s16x8 vf = __builtin_shufflevector(v0, v1, 0, 1, 2, 3, 4, 5, 6, 7);
      kv = MFMA(kf, vf, kv);
      zz = MFMA(kf, ones, zz);
    }
    float* kvp = (float*)(ws + WS_KVP) + ((size_t)rb * 32 + head) * 1024;
    *(f32x4*)(kvp + (et * 16 + tok) * 32 + dt * 16 + qq * 4) = kv;
    if (et == 0 && tok == 0) *(f32x4*)((float*)(ws + WS_ZP) + ((size_t)rb * 32 + head) * 32 + dt * 16 + qq * 4) = zz;
  } else {
    G1_HSTORE(0, 2, f0 + (2 * grp) * 16);
    G1_HSTORE(1, 3, f0 + (2 * grp + 1) * 16);
    G1_HSTORE(4, 6, f0 + (4 + 2 * grp) * 16);
    G1_HSTORE(5, 7, f0 + (5 + 2 * grp) * 16);
    G1_HSTORE(8, 9, f0 + (8 + grp) * 16);
  }
  {
    float s = 0.f;
#pragma unroll
    for (int i = 0; i < 4; ++i) {
      unsigned xa0 = gx0[i], wa0 = gw0[i], xa1 = gx1[i], wa1 = gw1[i];
      s += bfr(__builtin_bit_cast(float, xa0 << 16) * __builtin_bit_cast(float, wa0 << 16));
      s += bfr(__builtin_bit_cast(float, xa0 & 0xffff0000u) * __builtin_bit_cast(float, wa0 & 0xffff0000u));
      s += bfr(__builtin_bit_cast(float, xa1 << 16) * __builtin_bit_cast(float, wa1 << 16));
      s += bfr(__builtin_bit_cast(float, xa1 & 0xffff0000u) * __builtin_bit_cast(float, wa1 & 0xffff0000u));
    }
#pragma unroll
    for (int o = 32; o >= 1; o >>= 1) s += __shfl_xor(s, o);
    if (ln == 0) {
      float t = bfr(s);
      ((float*)(ws + WS_G))[grow] = bfr(1.0f / (1.0f + __expf(-t)));
    }
    if (b == 0 && tz < 160) *(unsigned*)(ws + WS_CNT + tz * 64) = 0u;
  }
  STAMPK(0, 3);
}

#define G2_N0 32
#define G2_STAGE 32768
#define G2_SCR (4 * G2_STAGE)
#define G2_LDS (G2_SCR + 4096)

__device__ __forceinline__ void glds16_sc1(const void* g, unsigned lds_addr) {
  __builtin_amdgcn_global_load_lds((const __attribute__((address_space(1))) unsigned*)g,
                                   (__attribute__((address_space(3))) unsigned*)(size_t)lds_addr, 16, 0, 16);
}
#define GLD4(dst, ptr) asm volatile("global_load_dwordx4 %0, %1, off" : "=v"(dst) : "v"(ptr) : "memory")
#define GST4_SC1(ptr, val) asm volatile("global_store_dwordx4 %0, %1, off sc1\n\ts_nop 1" ::"v"(ptr), "v"(val) : "memory")

__global__ __launch_bounds__(512, 2) void gemm2_kernel(const __bf16* __restrict__ wo, const __bf16* __restrict__ wd,
                                                       char* ws, __bf16* __restrict__ out) {
  extern __shared__ __attribute__((aligned(16))) char smem[];
  const unsigned lds0 = (unsigned)(size_t)(lds_char_p)smem;
  const int b = blockIdx.x;
  const int xcd = b & 7, idx = b >> 3;
  const bool isF = xcd & 1;
  const int rt = ((xcd >> 1) & 1) * 8 + (idx & 7), ctile = (xcd >> 2) * 4 + (idx >> 3);
  const int tile = rt * 8 + ctile;
  const int wave = __builtin_amdgcn_readfirstlane(threadIdx.x >> 6);
  const int l = threadIdx.x & 63;
  __bf16* Abuf = (__bf16*)(ws + WS_ABUF);
  const int s0 = isF ? G2_N0 : 0, n = isF ? 72 - G2_N0 : G2_N0;
  unsigned* pflag = (unsigned*)(ws + WS_CNT + 2048 + tile * 64);
  char* part = ws + WS_PART + (size_t)tile * 65536;
  STAMPK(1, 0);

  typedef __attribute__((address_space(3))) unsigned* lds_u32_p;
  const lds_u32_p kvcnt = (lds_u32_p)(size_t)(lds0 + G2_SCR + 2560);
  if (threadIdx.x == 0) *kvcnt = 0u;
  WAIT_L(0); BAR; SCHED;
  if (wave >= 4) {
    const int lw = wave - 4;
    const char* asrc = (const char*)Abuf + (size_t)rt * 72 * 16384 + lw * 4096;
    const unsigned voffA = (unsigned)((l >> 3) * 128 + (((l & 7) ^ (l >> 3)) * 16));
    const unsigned voffB0 = (unsigned)((lw * 16 + (l >> 3)) * 2048 + (((l & 7) ^ (((l >> 4) & 1) << 1)) * 16));
    const unsigned voffB1 = (voffB0 ^ 64u) + 8 * 2048;
    const char* bsrc_d = (const char*)(wd + ctile * 128);
    const char* bsrc_o = (const char*)(wo + ctile * 128);
#define G2_LOAD(GL, s, slot) { const unsigned _d = lds0 + (unsigned)(slot) * G2_STAGE; \
    const char* _a = asrc + (size_t)(s) * 16384; \
    const char* _b = (s) < 56 ? bsrc_d + (size_t)(s) * 131072 : bsrc_o + (size_t)((s) - 56) * 131072; \
    GL(_a + voffA, _d + lw * 4096); GL(_a + 1024 + voffA, _d + lw * 4096 + 1024); \
    GL(_a + 2048 + voffA, _d + lw * 4096 + 2048); GL(_a + 3072 + voffA, _d + lw * 4096 + 3072); \
    glds16(_b + voffB0, _d + 16384 + lw * 2048); glds16(_b + voffB1, _d + 16384 + lw * 2048 + 1024); \
    glds16(_b + 128 + voffB0, _d + 24576 + lw * 2048); glds16(_b + 128 + voffB1, _d + 24576 + lw * 2048 + 1024); }
    G2_LOAD(glds16, s0, 0); G2_LOAD(glds16, s0 + 1, 1); G2_LOAD(glds16, s0 + 2, 2); G2_LOAD(glds16, s0 + 3, 3);
    WAIT_V(24); BAR;
    int t = 0;
    const int nh = isF ? 56 - G2_N0 : G2_N0;
    for (; t + 4 < nh && t + 4 < n; ++t) { WAIT_V(16); BAR; G2_LOAD(glds16, s0 + t + 4, t & 3); }
    if (isF) {
      unsigned* cnt = (unsigned*)(ws + WS_CNT + (rt >> 1) * 256);
      while (__hip_atomic_load(cnt, __ATOMIC_RELAXED, __HIP_MEMORY_SCOPE_AGENT) < 32u) __builtin_amdgcn_s_sleep(4);
      for (; t + 4 < n; ++t) { WAIT_V(16); BAR; G2_LOAD(glds16_sc1, s0 + t + 4, t & 3); }
      WAIT_V(16); BAR;
      while (__hip_atomic_load(pflag, __ATOMIC_RELAXED, __HIP_MEMORY_SCOPE_AGENT) == 0u) __builtin_amdgcn_s_sleep(4);
#pragma unroll
      for (int i = 0; i < 8; ++i) glds16_sc1(part + lw * 8192 + i * 1024 + l * 16, lds0 + ((n - 4) & 3) * G2_STAGE + lw * 8192 + i * 1024);
      WAIT_V(16); BAR;
#pragma unroll
      for (int i = 0; i < 8; ++i) glds16_sc1(part + 32768 + lw * 8192 + i * 1024 + l * 16, lds0 + ((n - 3) & 3) * G2_STAGE + lw * 8192 + i * 1024);
      WAIT_V(16); BAR;
      BAR;
      WAIT_V(0); BAR;
    } else {
      WAIT_V(16); BAR;
      WAIT_V(8); BAR;
      WAIT_V(0); BAR;
      BAR;
      BAR;
      if (threadIdx.x == 256) __hip_atomic_store(pflag, 1u, __ATOMIC_RELAXED, __HIP_MEMORY_SCOPE_AGENT);
    }
    return;
  }

  const int wm = wave >> 1, wn = wave & 1;
  const int g4 = l >> 4, q4 = (l & 15) >> 2, pp = l & 3;
  const float* gv = (const float*)(ws + WS_G);
  u32x4 ov[4];
  __bf16* odst;
  {
    const int head = b & 31, r0 = (b >> 5) * 256 + wave * 64;
    const int e = l & 15;
    const float* kvp = (const float*)(ws + WS_KVP) + (size_t)head * 1024 + wave * 256 + l * 4;
    const float* zp = (const float*)(ws + WS_ZP) + (size_t)head * 32 + (l & 7) * 4;
    const __bf16* pq = (const __bf16*)(ws + WS_PQ) + PQIDX(r0 + e, head, g4 * 8);
    f32x4 kp[8], zq[8];
    s16x8 pf[4];
    float gr[4];
#pragma unroll
    for (int r = 0; r < 8; ++r) { kp[r] = *(const f32x4*)(kvp + (size_t)r * 32 * 1024); zq[r] = *(const f32x4*)(zp + (size_t)r * 1024); }
#pragma unroll
    for (int m = 0; m < 4; ++m) { pf[m] = *(const s16x8*)(pq + m * 16 * 32); gr[m] = gv[r0 + m * 16 + e]; }
    SCHED;
    f32x4 ks = ((kp[0] + kp[1]) + (kp[2] + kp[3])) + ((kp[4] + kp[5]) + (kp[6] + kp[7]));
    f32x4 zs = ((zq[0] + zq[1]) + (zq[2] + zq[3])) + ((zq[4] + zq[5]) + (zq[6] + zq[7]));
    *(__attribute__((address_space(3))) u32x2*)(size_t)(lds0 + G2_SCR + wave * 512 + l * 8) = u32x2{pack2(ks[0], ks[1]), pack2(ks[2], ks[3])};
    *(__attribute__((address_space(3))) u32x2*)(size_t)(lds0 + G2_SCR + 2048 + wave * 64 + (l & 7) * 8) = u32x2{pack2(zs[0], zs[1]), pack2(zs[2], zs[3])};
    STAMPK(1, 6);
    WAIT_L(0);
    if (l == 0) __hip_atomic_fetch_add(kvcnt, 1u, __ATOMIC_RELAXED, __HIP_MEMORY_SCOPE_WORKGROUP);
    while (__hip_atomic_load(kvcnt, __ATOMIC_RELAXED, __HIP_MEMORY_SCOPE_WORKGROUP) < 4u) __builtin_amdgcn_s_sleep(1);
    WAIT_L(0); SCHED;
    s16x8 kf0, kf1, zf;
    { const unsigned ka = lds0 + G2_SCR + (unsigned)(e * 64 + g4 * 16), za = lds0 + G2_SCR + 2048 + (unsigned)(wave * 64 + g4 * 16);
      ARD128(kf0, ka, 0); ARD128(kf1, ka, 1024); ARD128(zf, za, 0); WAIT_L(0); SCHED; }
#pragma unroll
    for (int m = 0; m < 4; ++m) {
      f32x4 z4 = {0, 0, 0, 0};
      f32x4 n0 = MFMA(kf0, pf[m], z4), n1 = MFMA(kf1, pf[m], z4), dn = MFMA(zf, pf[m], z4);
      const float rd = __builtin_amdgcn_rcpf(bfr(bfr(dn[0]) + 1e-6f)), g = gr[m];
      unsigned a0 = pack2(g * bfr(bfr(n0[0]) * rd), g * bfr(bfr(n0[1]) * rd)), a1 = pack2(g * bfr(bfr(n0[2]) * rd), g * bfr(bfr(n0[3]) * rd));
      unsigned c0 = pack2(g * bfr(bfr(n1[0]) * rd), g * bfr(bfr(n1[1]) * rd)), c1 = pack2(g * bfr(bfr(n1[2]) * rd), g * bfr(bfr(n1[3]) * rd));
      u32x2 s0_ = __builtin_amdgcn_permlane16_swap(a0, c0, false, false), s1_ = __builtin_amdgcn_permlane16_swap(a1, c1, false, false);
      ov[m] = u32x4{s0_[0], s1_[0], s0_[1], s1_[1]};
    }
    odst = Abuf + AIDX(r0, F_ + head * 32) + e * 64 + (g4 & 1) * 16 + (g4 >> 1) * 8;
  }
  STAMPK(1, 7);
  float og[4];
#pragma unroll
  for (int m = 0; m < 4; ++m) og[m] = bfr(1.0f - gv[rt * 128 + wm * 64 + m * 16 + (l & 15)]);

  const unsigned aOff0 = (unsigned)((wm * 64 + (l & 15)) * 128 + ((g4 ^ (l & 7)) * 16));
  const int sh = ((q4 >> 1) & 1) | ((g4 & 1) << 1);
  const unsigned bOffL = 16384u + (unsigned)(wn * 8192 + (8 * g4 + q4) * 128 + (pp >> 1) * 16 + (pp & 1) * 8);
  const unsigned bO0 = bOffL + (unsigned)((0 ^ sh) * 32), bO1 = bOffL + (unsigned)((1 ^ sh) * 32);
  const unsigned bO2 = bOffL + (unsigned)((2 ^ sh) * 32), bO3 = bOffL + (unsigned)((3 ^ sh) * 32);

  f32x4 acc[4][4];
#pragma unroll
  for (int m = 0; m < 4; ++m)
#pragma unroll
    for (int j = 0; j < 4; ++j) acc[m][j] = f32x4{0.f, 0.f, 0.f, 0.f};
  u32x4 aX[2][4], aY[2][4];
  u32x2 bX[2][8], bY[2][8];
#define G2_RDK(af, bf, sb, ks) { const unsigned _a = (sb) + (aOff0 ^ ((ks) * 64u)); \
    ARD128(af[ks][0], _a, 0); ARD128(af[ks][1], _a, 2048); ARD128(af[ks][2], _a, 4096); ARD128(af[ks][3], _a, 6144); \
    { const unsigned _b = (sb) + bO0; ATRF(bf[ks][0], bf[ks][1], _b, (ks) * 4096); } { const unsigned _b = (sb) + bO1; ATRF(bf[ks][2], bf[ks][3], _b, (ks) * 4096); } \
    { const unsigned _b = (sb) + bO2; ATRF(bf[ks][4], bf[ks][5], _b, (ks) * 4096); } { const unsigned _b = (sb) + bO3; ATRF(bf[ks][6], bf[ks][7], _b, (ks) * 4096); } }
#define G2_MMK(af, bf, ks) { _Pragma("unroll") for (int j = 0; j < 4; ++j) { \
    const s16x8 _f = __builtin_bit_cast(s16x8, __builtin_shufflevector(bf[ks][2 * j], bf[ks][2 * j + 1], 0, 1, 2, 3)); \
    _Pragma("unroll") for (int m = 0; m < 4; ++m) acc[m][j] = MFMA(_f, __builtin_bit_cast(s16x8, af[ks][m]), acc[m][j]); } }
#define G2_MM1(af, bf, ks, j, m) acc[m][j] = MFMA(__builtin_bit_cast(s16x8, __builtin_shufflevector(bf[ks][2 * (j)], bf[ks][2 * (j) + 1], 0, 1, 2, 3)), \
                                                  __builtin_bit_cast(s16x8, af[ks][m]), acc[m][j])
#define G2_HALF(afc, bfc, afn, bfn, sb, ks, RD) { \
    const unsigned _a = (sb) + (aOff0 ^ ((ks) * 64u)); \
    G2_MM1(afc, bfc, ks, 0, 0); if (RD) ARD128(afn[ks][0], _a, 0); SCHED; \
    G2_MM1(afc, bfc, ks, 0, 1); if (RD) ARD128(afn[ks][1], _a, 2048); SCHED; \
    G2_MM1(afc, bfc, ks, 0, 2); if (RD) ARD128(afn[ks][2], _a, 4096); SCHED; \
    G2_MM1(afc, bfc, ks, 0, 3); if (RD) ARD128(afn[ks][3], _a, 6144); SCHED; \
    G2_MM1(afc, bfc, ks, 1, 0); if (RD) ATR(bfn[ks][0], _b0, (ks) * 4096); SCHED; \
    G2_MM1(afc, bfc, ks, 1, 1); if (RD) ATR(bfn[ks][1], _b0, (ks) * 4096 + 512); SCHED; \
    G2_MM1(afc, bfc, ks, 1, 2); if (RD) ATR(bfn[ks][2], _b1, (ks) * 4096); SCHED; \
    G2_MM1(afc, bfc, ks, 1, 3); if (RD) ATR(bfn[ks][3], _b1, (ks) * 4096 + 512); SCHED; \
    G2_MM1(afc, bfc, ks, 2, 0); if (RD) ATR(bfn[ks][4], _b2, (ks) * 4096); SCHED; \
    G2_MM1(afc, bfc, ks, 2, 1); if (RD) ATR(bfn[ks][5], _b2, (ks) * 4096 + 512); SCHED; \
    G2_MM1(afc, bfc, ks, 2, 2); if (RD) ATR(bfn[ks][6], _b3, (ks) * 4096); SCHED; \
    G2_MM1(afc, bfc, ks, 2, 3); if (RD) ATR(bfn[ks][7], _b3, (ks) * 4096 + 512); SCHED; \
    G2_MM1(afc, bfc, ks, 3, 0); G2_MM1(afc, bfc, ks, 3, 1); G2_MM1(afc, bfc, ks, 3, 2); G2_MM1(afc, bfc, ks, 3, 3); SCHED; }
#define G2_STEP(t, afc, bfc, afn, bfn, RD, PF) { \
    WAIT_L(0); BAR; SCHED; \
    const unsigned _sb = lds0 + (unsigned)(((t) + 1) & 3) * G2_STAGE; \
    const unsigned _b0 = _sb + bO0, _b1 = _sb + bO1, _b2 = _sb + bO2, _b3 = _sb + bO3; \
    G2_HALF(afc, bfc, afn, bfn, _sb, 0, RD); \
    PF; SCHED; \
    G2_HALF(afc, bfc, afn, bfn, _sb, 1, RD); }

  BAR; SCHED;
  STAMPK(1, 1);
  G2_RDK(aX, bX, lds0, 0); G2_RDK(aX, bX, lds0, 1);
  const int nh = isF ? 56 - G2_N0 : G2_N0;
  G2_STEP(0, aX, bX, aY, bY, true, GST4_SC1(odst, ov[0]));
  G2_STEP(1, aY, bY, aX, bX, true, GST4_SC1(odst + 1024, ov[1]));
  G2_STEP(2, aX, bX, aY, bY, true, GST4_SC1(odst + 2048, ov[2]));
  G2_STEP(3, aY, bY, aX, bX, true, GST4_SC1(odst + 3072, ov[3]));
  int t = 4;
  for (; t < 8; t += 2) {
    G2_STEP(t, aX, bX, aY, bY, true, (void)0);
    G2_STEP(t + 1, aY, bY, aX, bX, true, (void)0);
  }
  WAIT_V(0);
  G2_STEP(t, aX, bX, aY, bY, true, (void)0);
  if (threadIdx.x == 0) __hip_atomic_fetch_add((unsigned*)(ws + WS_CNT + (b >> 5) * 256), 1u, __ATOMIC_RELAXED, __HIP_MEMORY_SCOPE_AGENT);
  G2_STEP(t + 1, aY, bY, aX, bX, true, (void)0);
  t += 2;
  for (; t < nh - (isF ? 0 : 2); t += 2) {
    G2_STEP(t, aX, bX, aY, bY, true, (void)0);
    G2_STEP(t + 1, aY, bY, aX, bX, true, (void)0);
  }
  STAMPK(1, 2);
  if (isF) {
#pragma unroll
    for (int m = 0; m < 4; ++m)
#pragma unroll
      for (int j = 0; j < 4; ++j) acc[m][j] *= og[m];
    for (; t < n - 2; t += 2) {
      G2_STEP(t, aX, bX, aY, bY, true, (void)0);
      G2_STEP(t + 1, aY, bY, aX, bX, true, (void)0);
    }
  }
  G2_STEP(t, aX, bX, aY, bY, true, (void)0);
  G2_STEP(t + 1, aY, bY, aX, bX, false, (void)0);
  STAMPK(1, 3);
  if (!isF) {
#pragma unroll
    for (int m = 0; m < 4; ++m)
#pragma unroll
      for (int j = 0; j < 4; ++j) { f32x4 v = acc[m][j] * og[m]; GST4_SC1(part + wave * 16384 + (m * 4 + j) * 1024 + l * 16, v); }
    WAIT_V(0); BAR;
    STAMPK(1, 4);
  } else {
    BAR; SCHED;
    STAMPK(1, 4);
    const unsigned pa = lds0 + (unsigned)((((n - 4) + (wave >> 1)) & 3) * G2_STAGE + (wave & 1) * 16384 + l * 16);
#pragma unroll
    for (int m = 0; m < 4; ++m) {
      f32x4 p0, p1, p2, p3;
      ARD128(p0, pa, (m * 4 + 0) * 1024); ARD128(p1, pa, (m * 4 + 1) * 1024); ARD128(p2, pa, (m * 4 + 2) * 1024); ARD128(p3, pa, (m * 4 + 3) * 1024);
      WAIT_L(0); SCHED;
      f32x4 v0 = acc[m][0] + p0, v1 = acc[m][1] + p1, v2 = acc[m][2] + p2, v3 = acc[m][3] + p3;
      __bf16* dst = out + (size_t)(rt * 128 + wm * 64 + m * 16 + (l & 15)) * D_ + ctile * 128 + wn * 64 + (g4 & 1) * 16 + (g4 >> 1) * 8;
      u32x2 x0 = __builtin_amdgcn_permlane16_swap(pack2(v0[0], v0[1]), pack2(v1[0], v1[1]), false, false);
      u32x2 x1 = __builtin_amdgcn_permlane16_swap(pack2(v0[2], v0[3]), pack2(v1[2], v1[3]), false, false);
      u32x2 y0 = __builtin_amdgcn_permlane16_swap(pack2(v2[0], v2[1]), pack2(v3[0], v3[1]), false, false);
      u32x2 y1 = __builtin_amdgcn_permlane16_swap(pack2(v2[2], v2[3]), pack2(v3[2], v3[3]), false, false);
      *(u32x4*)(dst) = u32x4{x0[0], x1[0], x0[1], x1[1]};
      *(u32x4*)(dst + 32) = u32x4{y0[0], y1[0], y0[1], y1[1]};
    }
  }
  STAMPK(1, 5);
}

extern "C" void kernel_launch(void* const* d_in, const int* in_sizes, int n_in, void* d_out, int out_size, void* d_ws,
                              size_t ws_size, hipStream_t stream) {
  const __bf16* x = (const __bf16*)d_in[0];
  const __bf16* wqkv = (const __bf16*)d_in[1];
  const __bf16* wo = (const __bf16*)d_in[2];
  const __bf16* wg = (const __bf16*)d_in[4];
  const __bf16* wu = (const __bf16*)d_in[5];
  const __bf16* wd = (const __bf16*)d_in[6];
  const __bf16* gw = (const __bf16*)d_in[7];
  hipFuncSetAttribute((const void*)gemm1_kernel, hipFuncAttributeMaxDynamicSharedMemorySize, G1_LDS);
  hipFuncSetAttribute((const void*)gemm2_kernel, hipFuncAttributeMaxDynamicSharedMemorySize, G2_LDS);
  gemm1_kernel<<<256, 512, G1_LDS, stream>>>(x, wqkv, wg, wu, gw, (char*)d_ws, wd, wo);
  gemm2_kernel<<<256, 512, G2_LDS, stream>>>(wo, wd, (char*)d_ws, (__bf16*)d_out);
}
```

```cpp
#include <hip/hip_runtime.h>
#include <hip/hip_bf16.h>
#include <stdint.h>

typedef __attribute__((ext_vector_type(8))) short s16x8;
typedef __attribute__((ext_vector_type(4))) short s16x4;
typedef __attribute__((ext_vector_type(4))) float f32x4;
typedef __attribute__((ext_vector_type(2))) float f32x2;
typedef __attribute__((ext_vector_type(2))) __bf16 bf16x2;
typedef __attribute__((ext_vector_type(2))) unsigned u32x2;
typedef __attribute__((ext_vector_type(4))) unsigned u32x4;
typedef __attribute__((address_space(3))) s16x4* lds_s16x4_p;
typedef __attribute__((address_space(3))) s16x8* lds_s16x8_p;
typedef __attribute__((address_space(3))) char* lds_char_p;

#define S_ 2048
#define D_ 1024
#define F_ 3584
#define AIDX(row, col) ((size_t)(((row) >> 7) * 72 + ((col) >> 6)) * 8192 + (size_t)(((row) & 127) * 64 + ((col) & 63)))
#define PQIDX(row, head, d) ((size_t)(head) * 65536 + (size_t)((row) * 32 + (d)))
#define KA_ 4608

#define WS_ABUF 0ull
#define WS_PQ   (WS_ABUF + (size_t)S_ * KA_ * 2)
#define WS_KVP  (WS_PQ + (size_t)S_ * D_ * 2)
#define WS_ZP   (WS_KVP + (size_t)8 * 32 * 1024 * 4)
#define WS_G    (WS_ZP + (size_t)8 * 32 * 32 * 4)
#define WS_CNT  (WS_G + (size_t)S_ * 4)
#define WS_PART (WS_CNT + 16384)

#define STAMPK(kid, i) do {} while (0)
#define WAIT_V(n) asm volatile("s_waitcnt vmcnt(" #n ")" ::: "memory")
#define WAIT_L(n) asm volatile("s_waitcnt lgkmcnt(" #n ")" ::: "memory")
#define BAR __builtin_amdgcn_s_barrier()
#define SCHED __builtin_amdgcn_sched_barrier(0)

__device__ __forceinline__ float bfr(float v) { return (float)(__bf16)v; }
__device__ __forceinline__ unsigned pack2(float a, float b) {
  f32x2 v = {a, b};
  return __builtin_bit_cast(unsigned, __builtin_convertvector(v, bf16x2));
}
__device__ __forceinline__ f32x2 unpk2(unsigned p) {
  return f32x2{__builtin_bit_cast(float, p << 16), __builtin_bit_cast(float, p & 0xffff0000u)};
}
__device__ __forceinline__ unsigned phi2(float a0, float a1) {
  const f32x2 t = unpk2(pack2(a0, a1));
  const f32x2 p = t + 1.0f, x = t * 1.4426950408889634f;
  const float e0 = __builtin_amdgcn_exp2f(x[0]), e1 = __builtin_amdgcn_exp2f(x[1]);
  const f32x2 r = unpk2(pack2(t[0] > 0.f ? p[0] : e0, t[1] > 0.f ? p[1] : e1)) + 1e-6f;
  return pack2(r[0], r[1]);
}
__device__ __forceinline__ unsigned glu2(float g0, float g1, float u0, float u1) {
  const f32x2 h = unpk2(pack2(g0, g1)) * unpk2(pack2(u0, u1));
  return pack2(h[0], h[1]);
}
__device__ __forceinline__ s16x8 lds_rd128(unsigned addr) { return *(lds_s16x8_p)(size_t)addr; }
__device__ __forceinline__ s16x4 lds_tr(unsigned addr) {
  return __builtin_amdgcn_ds_read_tr16_b64_v4i16((lds_s16x4_p)(size_t)addr);
}
__device__ __forceinline__ s16x8 lds_trfrag(unsigned addr) {
  s16x4 lo = lds_tr(addr), hi = lds_tr(addr + 512);
  return __builtin_shufflevector(lo, hi, 0, 1, 2, 3, 4, 5, 6, 7);
}
#define ARD128(dst, addr, off) asm volatile("ds_read_b128 %0, %1 offset:%2" : "=v"(dst) : "v"(addr), "i"(off) : "memory")
#define ATR(dst, addr, off) asm volatile("ds_read_b64_tr_b16 %0, %1 offset:%2" : "=v"(dst) : "v"(addr), "i"(off) : "memory")
#define ATRF(lo, hi, addr, off) { ATR(lo, addr, off); ATR(hi, addr, (off) + 512); }
#define CAT(lo, hi) __builtin_shufflevector(lo, hi, 0, 1, 2, 3, 4, 5, 6, 7)
__device__ __forceinline__ void glds16(const void* g, unsigned lds_addr) {
  __builtin_amdgcn_global_load_lds((const __attribute__((address_space(1))) unsigned*)g,
                                   (__attribute__((address_space(3))) unsigned*)(size_t)lds_addr, 16, 0, 0);
}
#define MFMA(a, b, c) __builtin_amdgcn_mfma_f32_16x16x32_bf16(a, b, c, 0, 0, 0)

#define G1_ASLOT 32768
#define G1_BBASE 65536
#define G1_BSLOT 20480
#define G1_PFB (G1_BBASE + 4 * G1_BSLOT)
#define G1_LDS (G1_PFB + 1024)
#define G1_PFW0 3

__global__ __launch_bounds__(512, 2) void gemm1_kernel(const __bf16* __restrict__ x, const __bf16* __restrict__ wqkv,
                                                       const __bf16* __restrict__ wg, const __bf16* __restrict__ wu,
                                                       const __bf16* __restrict__ gate_w, char* __restrict__ ws,
                                                       const __bf16* __restrict__ wd, const __bf16* __restrict__ wo) {
  extern __shared__ __attribute__((aligned(16))) char smem[];
  const unsigned lds0 = (unsigned)(size_t)(lds_char_p)smem;
  const int b = blockIdx.x;
  const int xcd = b & 7, idx = b >> 3, rb = idx & 7, ct = xcd * 4 + (idx >> 3);
  const bool isA = ct < 16;
  const int f0 = isA ? ct * 64 : 1024 + (ct - 16) * 160;
  const int wave = __builtin_amdgcn_readfirstlane(threadIdx.x >> 6);
  const int l = threadIdx.x & 63;
  const int grp = wave >> 2, mw = wave & 3;
  STAMPK(0, 0);

  const char* xa = (const char*)x + (size_t)(rb * 256 + wave * 32) * 2048;
  const unsigned voffA = (unsigned)((l >> 3) * 2048 + (((l & 7) ^ (l >> 3)) * 16));
  const int krow = 8 * mw + (l >> 3);
  const int cl = (l & 7) ^ ((((l >> 4) & 1) | ((mw & 1) << 1)) << 1);
  const int rs0 = isA ? 3072 : F_;
  const unsigned voffQ = (unsigned)(krow * rs0 * 2 + cl * 16);
  const unsigned voffF = (unsigned)(krow * F_ * 2 + cl * 16);
  const char* sb0 = isA ? (const char*)(wqkv + ct * 64) : (const char*)(wg + f0);
  const char* sb1 = isA ? (const char*)(wqkv + 1024 + ct * 64) : (const char*)(wu + f0);
  const char* sb2 = isA ? (const char*)(wqkv + 2048 + ct * 64) : (const char*)(wg + f0 + 64);
  const char* sb3 = isA ? (const char*)(wg + f0) : (const char*)(wu + f0 + 64);
  const char* p4 = isA ? (const char*)(wu + f0 + cl * 8)
                       : (cl < 4 ? (const char*)(wg + f0 + 128 + cl * 8) : (const char*)(wu + f0 + 128 + (cl - 4) * 8));
  p4 += (size_t)krow * F_ * 2;
  const size_t pst0 = (size_t)32 * rs0 * 2, pstF = (size_t)32 * F_ * 2;

#define G1_LDA(kt, i) glds16(xa + (size_t)(kt) * 128 + (i) * 16384 + voffA, lds0 + ((kt) & 1) * G1_ASLOT + wave * 4096 + (i) * 1024)
#define G1_BDST(p, s) (lds0 + G1_BBASE + ((p) & 3) * G1_BSLOT + (s) * 4096 + mw * 1024)
#define G1_LDB0(p) glds16(sb0 + (size_t)(p) * pst0 + voffQ, G1_BDST(p, 0))
#define G1_LDB1(p) glds16(sb1 + (size_t)(p) * pst0 + voffQ, G1_BDST(p, 1))
#define G1_LDB2(p) glds16(sb2 + (size_t)(p) * pst0 + voffQ, G1_BDST(p, 2))
#define G1_LDB3(p) glds16(sb3 + (size_t)(p) * pstF + voffF, G1_BDST(p, 3))
#define G1_LDB4(p) glds16(p4 + (size_t)(p) * pstF, G1_BDST(p, 4))

#define G1_PF(sl) { int _l = threadIdx.x & 63; asm volatile("" : "+v"(_l)); \
    const int _s = (sl) + mw, _c = _s > 8 ? 8 : _s; \
    const char* _p = (_c < 7 ? (const char*)wd + (size_t)_c * 1048576 : (const char*)wo + (size_t)(_c - 7) * 1048576) + (size_t)(unsigned)((b * 64 + _l) * 64); \
    __builtin_amdgcn_global_load_lds((const __attribute__((address_space(1))) unsigned*)_p, \
                                     (__attribute__((address_space(3))) unsigned*)(size_t)(lds0 + G1_PFB + mw * 256), 4, 0, 0); }

  const int g4 = l >> 4, q4 = (l & 15) >> 2, pp = l & 3;
  unsigned aAddr0 = lds0 + (unsigned)((mw * 64 + (l & 15)) * 128 + ((g4 ^ (l & 7)) * 16));
  unsigned aAddr1 = aAddr0 ^ 64u;
  const int sh = ((q4 >> 1) & 1) | ((g4 & 1) << 1);
  const unsigned bl = lds0 + G1_BBASE + (unsigned)((8 * g4 + q4) * 128 + (pp >> 1) * 16 + (pp & 1) * 8);
  unsigned bA0 = bl + (unsigned)(((2 * grp) ^ sh) * 32);
  unsigned bA1 = bl + (unsigned)(((2 * grp + 1) ^ sh) * 32);
  unsigned bQ0 = bl + 4 * 4096 + (unsigned)(((isA ? 2 * grp : grp) ^ sh) * 32);
  unsigned bQ1 = bl + 4 * 4096 + (unsigned)(((isA ? 2 * grp + 1 : grp + 2) ^ sh) * 32);
  int bdelta = 2 * G1_BSLOT;

  f32x4 acc[4][10];
#pragma unroll
  for (int m = 0; m < 4; ++m)
#pragma unroll
    for (int j = 0; j < 10; ++j) acc[m][j] = f32x4{0.f, 0.f, 0.f, 0.f};
  s16x8 af[2][4];
  s16x4 bl_[5], bh_[5];

#define G1_RDA() { ARD128(af[0][0], aAddr0, 0); ARD128(af[0][1], aAddr0, 2048); ARD128(af[0][2], aAddr0, 4096); ARD128(af[0][3], aAddr0, 6144); \
    ARD128(af[1][0], aAddr1, 0); ARD128(af[1][1], aAddr1, 2048); ARD128(af[1][2], aAddr1, 4096); ARD128(af[1][3], aAddr1, 6144); }
#define G1_RDB_LO(ps) { ATRF(bl_[0], bh_[0], bA0, (ps) * G1_BSLOT); ATRF(bl_[1], bh_[1], bA1, (ps) * G1_BSLOT); \
    ATRF(bl_[2], bh_[2], bA0, (ps) * G1_BSLOT + 4096); ATRF(bl_[3], bh_[3], bA1, (ps) * G1_BSLOT + 4096); \
    ATRF(bl_[4], bh_[4], bA0, (ps) * G1_BSLOT + 8192); }
#define G1_RDB_HI(ps) { ATRF(bl_[0], bh_[0], bA1, (ps) * G1_BSLOT + 8192); ATRF(bl_[1], bh_[1], bA0, (ps) * G1_BSLOT + 12288); \
    ATRF(bl_[2], bh_[2], bA1, (ps) * G1_BSLOT + 12288); ATRF(bl_[3], bh_[3], bQ0, (ps) * G1_BSLOT); \
    ATRF(bl_[4], bh_[4], bQ1, (ps) * G1_BSLOT); }
#define G1_MMA(ks, j0) { WAIT_L(0); SCHED; __builtin_amdgcn_s_setprio(1); \
    _Pragma("unroll") for (int j = 0; j < 5; ++j) { const s16x8 bfj = CAT(bl_[j], bh_[j]); _Pragma("unroll") for (int m = 0; m < 4; ++m) \
      acc[m][(j0) + j] = MFMA(bfj, af[ks][m], acc[m][(j0) + j]); } \
    __builtin_amdgcn_s_setprio(0); SCHED; }
#define G1_TOGGLE() { aAddr0 ^= G1_ASLOT; aAddr1 ^= G1_ASLOT; bA0 += bdelta; bA1 += bdelta; bQ0 += bdelta; bQ1 += bdelta; bdelta = -bdelta; }

  auto epilogue = [&](const int grp) __attribute__((always_inline)) {
  SCHED;
  STAMPK(0, 2);
  int tz = threadIdx.x; asm volatile("" : "+v"(tz));
  const int ln = tz & 63, tok = ln & 15, qq = ln >> 4;
  const int row0 = rb * 256 + mw * 64;
  __bf16* Abuf = (__bf16*)(ws + WS_ABUF);
#define G1_H0(m, jg, ju) glu2(acc[m][jg][0], acc[m][jg][1], acc[m][ju][0], acc[m][ju][1])
#define G1_H1(m, jg, ju) glu2(acc[m][jg][2], acc[m][jg][3], acc[m][ju][2], acc[m][ju][3])
#define G1_SWAP16(a0, a1, c0, c1) ({ u32x2 _x = __builtin_amdgcn_permlane16_swap(a0, c0, false, false), _y = __builtin_amdgcn_permlane16_swap(a1, c1, false, false); \
      u32x4{_x[0], _y[0], _x[1], _y[1]}; })
#define G1_STM 0
#define G1_ST(ptr, val) *(u32x4*)(ptr) = (val)
#define G1_HST2(m, jg0, ju0, jg1, ju1, fcol) \
      G1_ST(Abuf + AIDX(row0 + (m) * 16, fcol) + tok * 64 + (qq & 1) * 16 + (qq >> 1) * 8, \
            G1_SWAP16(G1_H0(m, jg0, ju0), G1_H1(m, jg0, ju0), G1_H0(m, jg1, ju1), G1_H1(m, jg1, ju1)))
#define G1_HSTORE2(jg0, ju0, jg1, ju1, fcol) _Pragma("unroll") for (int m = 0; m < 4; ++m) { G1_HST2(m, jg0, ju0, jg1, ju1, fcol); }
#define G1_HSTORE1(jg, ju, fcol) _Pragma("unroll") for (int m = 0; m < 4; m += 2) { \
      G1_ST(Abuf + AIDX(row0 + m * 16, fcol) + ((qq & 1) * 16 + tok) * 64 + (qq >> 1) * 8, \
            G1_SWAP16(G1_H0(m, jg, ju), G1_H1(m, jg, ju), G1_H0(m + 1, jg, ju), G1_H1(m + 1, jg, ju))); }

  if (isA) {
    const int head = 2 * ct + grp;
    __bf16* pq = (__bf16*)(ws + WS_PQ);
    const unsigned kvb = lds0 + (unsigned)(grp * 256 * 136 + (mw * 64 + tok) * 136 + qq * 8);
#pragma unroll
    for (int m = 0; m < 4; ++m) {
      G1_HST2(m, 6, 8, 7, 9, f0 + grp * 32);
      SCHED;
#pragma unroll
      for (int jt = 0; jt < 2; ++jt) {
        u32x2 vk = {phi2(acc[m][2 + jt][0], acc[m][2 + jt][1]), phi2(acc[m][2 + jt][2], acc[m][2 + jt][3])};
        *(__attribute__((address_space(3))) u32x2*)(size_t)(kvb + m * 16 * 136 + jt * 32) = vk;
        u32x2 vv = {pack2(acc[m][4 + jt][0], acc[m][4 + jt][1]), pack2(acc[m][4 + jt][2], acc[m][4 + jt][3])};
        *(__attribute__((address_space(3))) u32x2*)(size_t)(kvb + m * 16 * 136 + 64 + jt * 32) = vv;
      }
      SCHED;
    }
    STAMPK(0, 5);
    WAIT_L(0); BAR; asm volatile("" ::: "memory"); SCHED;
    STAMPK(0, 6);
    const int dt = (wave >> 1) & 1, et = wave & 1;
    const int tq = (ln & 15) >> 2, tp = ln & 3;
    const unsigned rbase = lds0 + (unsigned)(grp * 256 * 136 + (8 * qq + tq) * 136 + tp * 8);
    f32x4 kv = {0.f, 0.f, 0.f, 0.f}, zz = {0.f, 0.f, 0.f, 0.f};
    const short one = 0x3F80;
    const s16x8 ones = {one, one, one, one, one, one, one, one};
#pragma unroll
    for (int s = 0; s < 8; ++s) {
      s16x4 k0 = lds_tr(rbase + s * 32 * 136 + dt * 32), k1 = lds_tr(rbase + s * 32 * 136 + 4 * 136 + dt * 32);
      s16x4 v0 = lds_tr(rbase + s * 32 * 136 + 64 + et * 32), v1 = lds_tr(rbase + s * 32 * 136 + 4 * 136 + 64 + et * 32);
      s16x8 kf = __builtin_shufflevector(k0, k1, 0, 1, 2, 3, 4, 5, 6, 7);
      s16x8 vf = __builtin_shufflevector(v0, v1, 0, 1, 2, 3, 4, 5, 6, 7);
      kv = MFMA(kf, vf, kv);
      zz = MFMA(kf, ones, zz);
    }
    SCHED;
#pragma unroll
    for (int m = 0; m < 4; ++m)
      G1_ST(pq + PQIDX(row0 + m * 16 + tok, head, (qq & 1) * 16 + (qq >> 1) * 8),
            G1_SWAP16(phi2(acc[m][0][0], acc[m][0][1]), phi2(acc[m][0][2], acc[m][0][3]), phi2(acc[m][1][0], acc[m][1][1]), phi2(acc[m][1][2], acc[m][1][3])));
    SCHED;
    float* kvp = (float*)(ws + WS_KVP) + ((size_t)rb * 32 + head) * 1024;
    *(f32x4*)(kvp + (et * 16 + tok) * 32 + dt * 16 + qq * 4) = kv;
    if (et == 0 && tok == 0) *(f32x4*)((float*)(ws + WS_ZP) + ((size_t)rb * 32 + head) * 32 + dt * 16 + qq * 4) = zz;
  } else {
    const int grow = ((ct - 16) * 8 + rb) * 16 + wave * 2 + (ln >> 5);
    G1_HSTORE2(0, 2, 1, 3, f0 + grp * 32);
    SCHED;
    u32x4 gx[4], gw[4];
#pragma unroll
    for (int i = 0; i < 4; ++i) { gx[i] = *(const u32x4*)(x + (size_t)grow * D_ + i * 256 + (ln & 31) * 8); gw[i] = *(const u32x4*)(gate_w + i * 256 + (ln & 31) * 8); }
    SCHED;
    G1_HSTORE2(4, 6, 5, 7, f0 + 64 + grp * 32);
    G1_HSTORE1(8, 9, f0 + 128 + grp * 16);
    SCHED;
    f32x2 s2 = {0.f, 0.f};
#pragma unroll
    for (int i = 0; i < 4; ++i)
#pragma unroll
      for (int j = 0; j < 4; ++j) { const f32x2 pr = unpk2(gx[i][j]) * unpk2(gw[i][j]); s2 += unpk2(pack2(pr[0], pr[1])); }
    float sg = s2[0] + s2[1];
#define G1_DPPADD(ctrl) sg += __builtin_bit_cast(float, __builtin_amdgcn_update_dpp(0, __builtin_bit_cast(int, sg), ctrl, 0xf, 0xf, true))
    G1_DPPADD(0xB1); G1_DPPADD(0x4E); G1_DPPADD(0x141); G1_DPPADD(0x140);
    sg += __shfl_xor(sg, 16);
    if ((ln & 31) == 0) ((float*)(ws + WS_G))[grow] = bfr(1.0f / (1.0f + __expf(-bfr(sg))));
    if (b == 4 && tz < 160) *(unsigned*)(ws + WS_CNT + tz * 64) = 0u;
    STAMPK(0, 5); STAMPK(0, 6);
  }
  STAMPK(0, 7);
  };

  if (grp == 0) {
    G1_LDA(0, 0); G1_LDA(0, 1); G1_LDA(0, 2); G1_LDA(0, 3);
    G1_LDB0(0); G1_LDB1(0); G1_LDB2(0); G1_LDB3(0); G1_LDB4(0);
    G1_LDA(1, 0); G1_LDA(1, 1); G1_LDA(1, 2); G1_LDA(1, 3);
    G1_LDB0(2); G1_LDB1(2); G1_LDB2(2);
    WAIT_V(7); BAR; SCHED;
    STAMPK(0, 1);
#define G1_ITER0(i, LB0, LA, LB3, WEND) { \
      G1_RDB_LO(0); G1_RDA(); if (LB0) { G1_LDB3(2 * (i) + 2); G1_LDB4(2 * (i) + 2); } SCHED; BAR; G1_MMA(0, 0); BAR; SCHED; \
      if (LA && (unsigned)((i) - G1_PFW0) < 3u) { G1_PF(4 * ((i) - G1_PFW0)); SCHED; } G1_RDB_HI(0); SCHED; BAR; G1_MMA(0, 5); BAR; SCHED; \
      G1_RDB_LO(1); if (LA) { G1_LDA((i) + 2, 0); G1_LDA((i) + 2, 1); G1_LDA((i) + 2, 2); G1_LDA((i) + 2, 3); } SCHED; BAR; G1_MMA(1, 0); BAR; SCHED; \
      G1_RDB_HI(1); if (LB3) { G1_LDB0(2 * (i) + 4); G1_LDB1(2 * (i) + 4); G1_LDB2(2 * (i) + 4); } SCHED; BAR; G1_MMA(1, 5); WEND; BAR; SCHED; \
      G1_TOGGLE(); }
    for (int i = 0; i < 14; ++i) G1_ITER0(i, true, true, true, if ((unsigned)(i - G1_PFW0) < 3u) WAIT_V(8); else WAIT_V(7));
    G1_ITER0(14, true, false, false, WAIT_V(0));
    G1_ITER0(15, false, false, false, (void)0);
    BAR;
    epilogue(0);
  } else {
    G1_LDA(0, 0); G1_LDA(0, 1); G1_LDA(0, 2); G1_LDA(0, 3);
    G1_LDB0(1); G1_LDB1(1); G1_LDB2(1); G1_LDB3(1); G1_LDB4(1);
    G1_LDA(1, 0); G1_LDA(1, 1); G1_LDA(1, 2); G1_LDA(1, 3);
    WAIT_V(9); BAR; SCHED;
    BAR; SCHED;
#define G1_ITER1(i, LB, LA, W1, W3) { \
      G1_RDB_LO(0); G1_RDA(); if (LB) { G1_LDB0(2 * (i) + 3); G1_LDB1(2 * (i) + 3); } SCHED; BAR; G1_MMA(0, 0); BAR; SCHED; \
      G1_RDB_HI(0); if (LB) { G1_LDB2(2 * (i) + 3); G1_LDB3(2 * (i) + 3); G1_LDB4(2 * (i) + 3); } SCHED; W1; BAR; G1_MMA(0, 5); BAR; SCHED; \
      G1_RDB_LO(1); if (LA) { G1_LDA((i) + 2, 0); G1_LDA((i) + 2, 1); } SCHED; BAR; G1_MMA(1, 0); BAR; SCHED; \
      G1_RDB_HI(1); if (LA) { G1_LDA((i) + 2, 2); G1_LDA((i) + 2, 3); } SCHED; W3; BAR; G1_MMA(1, 5); BAR; SCHED; \
      G1_TOGGLE(); }
    for (int i = 0; i < 14; ++i) G1_ITER1(i, true, true, WAIT_V(9), WAIT_V(9));
    G1_ITER1(14, true, false, WAIT_V(9), WAIT_V(5));
    G1_ITER1(15, false, false, WAIT_V(0), (void)0);
    epilogue(1);
  }

  STAMPK(0, 3);
}

#define G2_N0 32
#define G2_STAGE 32768
#define G2_SCR (4 * G2_STAGE)
#define G2_LDS (G2_SCR + 4096)

__device__ __forceinline__ void glds16_sc1(const void* g, unsigned lds_addr) {
  __builtin_amdgcn_global_load_lds((const __attribute__((address_space(1))) unsigned*)g,
                                   (__attribute__((address_space(3))) unsigned*)(size_t)lds_addr, 16, 0, 16);
}
#define GLD4(dst, ptr) asm volatile("global_load_dwordx4 %0, %1, off" : "=v"(dst) : "v"(ptr) : "memory")
#define GST4_SC1(ptr, val) asm volatile("global_store_dwordx4 %0, %1, off sc1\n\ts_nop 1" ::"v"(ptr), "v"(val) : "memory")

__global__ __launch_bounds__(512, 2) void gemm2_kernel(const __bf16* __restrict__ wo, const __bf16* __restrict__ wd,
                                                       char* ws, __bf16* __restrict__ out) {
  extern __shared__ __attribute__((aligned(16))) char smem[];
  const unsigned lds0 = (unsigned)(size_t)(lds_char_p)smem;
  const int b = blockIdx.x;
  const int xcd = b & 7, idx = b >> 3;
  const bool isF = xcd & 1;
  const int rt = ((xcd >> 1) & 1) * 8 + (idx & 7), ctile = (xcd >> 2) * 4 + (idx >> 3);
  const int tile = rt * 8 + ctile;
  const int wave = __builtin_amdgcn_readfirstlane(threadIdx.x >> 6);
  const int l = threadIdx.x & 63;
  __bf16* Abuf = (__bf16*)(ws + WS_ABUF);
  const int s0 = isF ? G2_N0 : 0, n = isF ? 72 - G2_N0 : G2_N0;
  unsigned* pflag = (unsigned*)(ws + WS_CNT + 2048 + tile * 64);
  char* part = ws + WS_PART + (size_t)tile * 65536;
  STAMPK(1, 0);

  typedef __attribute__((address_space(3))) unsigned* lds_u32_p;
  const lds_u32_p kvcnt = (lds_u32_p)(size_t)(lds0 + G2_SCR + 2560);
  if (threadIdx.x == 0) *kvcnt = 0u;
  WAIT_L(0); BAR; SCHED;
  if (wave >= 4) {
    const int lw = wave - 4;
    const char* asrc = (const char*)Abuf + (size_t)rt * 72 * 16384 + lw * 4096;
    const unsigned voffA = (unsigned)((l >> 3) * 128 + (((l & 7) ^ (l >> 3)) * 16));
    const unsigned voffB0 = (unsigned)((lw * 16 + (l >> 3)) * 2048 + (((l & 7) ^ (((l >> 4) & 1) << 1)) * 16));
    const unsigned voffB1 = (voffB0 ^ 64u) + 8 * 2048;
    const char* bsrc_d = (const char*)(wd + ctile * 128);
    const char* bsrc_o = (const char*)(wo + ctile * 128);
#define G2_LOAD(GL, s, slot) { const unsigned _d = lds0 + (unsigned)(slot) * G2_STAGE; \
    const char* _a = asrc + (size_t)(s) * 16384; \
    const char* _b = (s) < 56 ? bsrc_d + (size_t)(s) * 131072 : bsrc_o + (size_t)((s) - 56) * 131072; \
    GL(_a + voffA, _d + lw * 4096); GL(_a + 1024 + voffA, _d + lw * 4096 + 1024); \
    GL(_a + 2048 + voffA, _d + lw * 4096 + 2048); GL(_a + 3072 + voffA, _d + lw * 4096 + 3072); \
    glds16(_b + voffB0, _d + 16384 + lw * 2048); glds16(_b + voffB1, _d + 16384 + lw * 2048 + 1024); \
    glds16(_b + 128 + voffB0, _d + 24576 + lw * 2048); glds16(_b + 128 + voffB1, _d + 24576 + lw * 2048 + 1024); }
    G2_LOAD(glds16, s0, 0); G2_LOAD(glds16, s0 + 1, 1); G2_LOAD(glds16, s0 + 2, 2); G2_LOAD(glds16, s0 + 3, 3);
    WAIT_V(24); BAR;
    int t = 0;
    const int nh = isF ? 56 - G2_N0 : G2_N0;
    for (; t + 4 < nh && t + 4 < n; ++t) { WAIT_V(16); BAR; G2_LOAD(glds16, s0 + t + 4, t & 3); }
    if (isF) {
      unsigned* cnt = (unsigned*)(ws + WS_CNT + (rt >> 1) * 256);
      while (__hip_atomic_load(cnt, __ATOMIC_RELAXED, __HIP_MEMORY_SCOPE_AGENT) < 32u) __builtin_amdgcn_s_sleep(4);
      for (; t + 4 < n; ++t) { WAIT_V(16); BAR; G2_LOAD(glds16_sc1, s0 + t + 4, t & 3); }
      WAIT_V(16); BAR;
      while (__hip_atomic_load(pflag, __ATOMIC_RELAXED, __HIP_MEMORY_SCOPE_AGENT) == 0u) __builtin_amdgcn_s_sleep(4);
#pragma unroll
      for (int i = 0; i < 8; ++i) glds16_sc1(part + lw * 8192 + i * 1024 + l * 16, lds0 + ((n - 4) & 3) * G2_STAGE + lw * 8192 + i * 1024);
      WAIT_V(16); BAR;
#pragma unroll
      for (int i = 0; i < 8; ++i) glds16_sc1(part + 32768 + lw * 8192 + i * 1024 + l * 16, lds0 + ((n - 3) & 3) * G2_STAGE + lw * 8192 + i * 1024);
      WAIT_V(16); BAR;
      BAR;
      WAIT_V(0); BAR;
    } else {
      WAIT_V(16); BAR;
      WAIT_V(8); BAR;
      WAIT_V(0); BAR;
      BAR;
      BAR;
      if (threadIdx.x == 256) __hip_atomic_store(pflag, 1u, __ATOMIC_RELAXED, __HIP_MEMORY_SCOPE_AGENT);
    }
    return;
  }

  const int wm = wave >> 1, wn = wave & 1;
  const int g4 = l >> 4, q4 = (l & 15) >> 2, pp = l & 3;
  const float* gv = (const float*)(ws + WS_G);
  u32x4 ov[4];
  __bf16* odst;
  {
    const int head = b & 31, r0 = (b >> 5) * 256 + wave * 64;
    const int e = l & 15;
    const float* kvp = (const float*)(ws + WS_KVP) + (size_t)head * 1024 + wave * 256 + l * 4;
    const float* zp = (const float*)(ws + WS_ZP) + (size_t)head * 32 + (l & 7) * 4;
    const __bf16* pq = (const __bf16*)(ws + WS_PQ) + PQIDX(r0 + e, head, g4 * 8);
    f32x4 kp[8], zq[8];
    s16x8 pf[4];
    float gr[4];
#pragma unroll
    for (int r = 0; r < 8; ++r) { kp[r] = *(const f32x4*)(kvp + (size_t)r * 32 * 1024); zq[r] = *(const f32x4*)(zp + (size_t)r * 1024); }
#pragma unroll
    for (int m = 0; m < 4; ++m) { pf[m] = *(const s16x8*)(pq + m * 16 * 32); gr[m] = gv[r0 + m * 16 + e]; }
    SCHED;
    f32x4 ks = ((kp[0] + kp[1]) + (kp[2] + kp[3])) + ((kp[4] + kp[5]) + (kp[6] + kp[7]));
    f32x4 zs = ((zq[0] + zq[1]) + (zq[2] + zq[3])) + ((zq[4] + zq[5]) + (zq[6] + zq[7]));
    *(__attribute__((address_space(3))) u32x2*)(size_t)(lds0 + G2_SCR + wave * 512 + l * 8) = u32x2{pack2(ks[0], ks[1]), pack2(ks[2], ks[3])};
    *(__attribute__((address_space(3))) u32x2*)(size_t)(lds0 + G2_SCR + 2048 + wave * 64 + (l & 7) * 8) = u32x2{pack2(zs[0], zs[1]), pack2(zs[2], zs[3])};
    STAMPK(1, 6);
    WAIT_L(0);
    if (l == 0) __hip_atomic_fetch_add(kvcnt, 1u, __ATOMIC_RELAXED, __HIP_MEMORY_SCOPE_WORKGROUP);
    while (__hip_atomic_load(kvcnt, __ATOMIC_RELAXED, __HIP_MEMORY_SCOPE_WORKGROUP) < 4u) __builtin_amdgcn_s_sleep(1);
    WAIT_L(0); SCHED;
    s16x8 kf0, kf1, zf;
    { const unsigned ka = lds0 + G2_SCR + (unsigned)(e * 64 + g4 * 16), za = lds0 + G2_SCR + 2048 + (unsigned)(wave * 64 + g4 * 16);
      ARD128(kf0, ka, 0); ARD128(kf1, ka, 1024); ARD128(zf, za, 0); WAIT_L(0); SCHED; }
#pragma unroll
    for (int m = 0; m < 4; ++m) {
      f32x4 z4 = {0, 0, 0, 0};
      f32x4 n0 = MFMA(kf0, pf[m], z4), n1 = MFMA(kf1, pf[m], z4), dn = MFMA(zf, pf[m], z4);
      const float rd = __builtin_amdgcn_rcpf(bfr(bfr(dn[0]) + 1e-6f)), g = gr[m];
#define G2_O2(a, c) ({ f32x2 _q = unpk2(pack2(a, c)) * rd; _q = unpk2(pack2(_q[0], _q[1])) * g; pack2(_q[0], _q[1]); })
      unsigned a0 = G2_O2(n0[0], n0[1]), a1 = G2_O2(n0[2], n0[3]), c0 = G2_O2(n1[0], n1[1]), c1 = G2_O2(n1[2], n1[3]);
      u32x2 s0_ = __builtin_amdgcn_permlane16_swap(a0, c0, false, false), s1_ = __builtin_amdgcn_permlane16_swap(a1, c1, false, false);
      ov[m] = u32x4{s0_[0], s1_[0], s0_[1], s1_[1]};
    }
    odst = Abuf + AIDX(r0, F_ + head * 32) + e * 64 + (g4 & 1) * 16 + (g4 >> 1) * 8;
  }
  STAMPK(1, 7);
  float og[4];
#pragma unroll
  for (int m = 0; m < 4; ++m) og[m] = bfr(1.0f - gv[rt * 128 + wm * 64 + m * 16 + (l & 15)]);

  const unsigned aOff0 = (unsigned)((wm * 64 + (l & 15)) * 128 + ((g4 ^ (l & 7)) * 16));
  const int sh = ((q4 >> 1) & 1) | ((g4 & 1) << 1);
  const unsigned bOffL = 16384u + (unsigned)(wn * 8192 + (8 * g4 + q4) * 128 + (pp >> 1) * 16 + (pp & 1) * 8);
  const unsigned bO0 = bOffL + (unsigned)((0 ^ sh) * 32), bO1 = bOffL + (unsigned)((1 ^ sh) * 32);
  const unsigned bO2 = bOffL + (unsigned)((2 ^ sh) * 32), bO3 = bOffL + (unsigned)((3 ^ sh) * 32);

  f32x4 acc[4][4];
#pragma unroll
  for (int m = 0; m < 4; ++m)
#pragma unroll
    for (int j = 0; j < 4; ++j) acc[m][j] = f32x4{0.f, 0.f, 0.f, 0.f};
  u32x4 aX[2][4], aY[2][4];
  u32x2 bX[2][8], bY[2][8];
#define G2_RDK(af, bf, sb, ks) { const unsigned _a = (sb) + (aOff0 ^ ((ks) * 64u)); \
    ARD128(af[ks][0], _a, 0); ARD128(af[ks][1], _a, 2048); ARD128(af[ks][2], _a, 4096); ARD128(af[ks][3], _a, 6144); \
    { const unsigned _b = (sb) + bO0; ATRF(bf[ks][0], bf[ks][1], _b, (ks) * 4096); } { const unsigned _b = (sb) + bO1; ATRF(bf[ks][2], bf[ks][3], _b, (ks) * 4096); } \
    { const unsigned _b = (sb) + bO2; ATRF(bf[ks][4], bf[ks][5], _b, (ks) * 4096); } { const unsigned _b = (sb) + bO3; ATRF(bf[ks][6], bf[ks][7], _b, (ks) * 4096); } }
#define G2_MMK(af, bf, ks) { _Pragma("unroll") for (int j = 0; j < 4; ++j) { \
    const s16x8 _f = __builtin_bit_cast(s16x8, __builtin_shufflevector(bf[ks][2 * j], bf[ks][2 * j + 1], 0, 1, 2, 3)); \
    _Pragma("unroll") for (int m = 0; m < 4; ++m) acc[m][j] = MFMA(_f, __builtin_bit_cast(s16x8, af[ks][m]), acc[m][j]); } }
#define G2_MM1(af, bf, ks, j, m) acc[m][j] = MFMA(__builtin_bit_cast(s16x8, __builtin_shufflevector(bf[ks][2 * (j)], bf[ks][2 * (j) + 1], 0, 1, 2, 3)), \
                                                  __builtin_bit_cast(s16x8, af[ks][m]), acc[m][j])
#define G2_HALF(afc, bfc, afn, bfn, sb, ks, RD) { \
    const unsigned _a = (sb) + (aOff0 ^ ((ks) * 64u)); \
    G2_MM1(afc, bfc, ks, 0, 0); if (RD) ARD128(afn[ks][0], _a, 0); SCHED; \
    G2_MM1(afc, bfc, ks, 0, 1); if (RD) ARD128(afn[ks][1], _a, 2048); SCHED; \
    G2_MM1(afc, bfc, ks, 0, 2); if (RD) ARD128(afn[ks][2], _a, 4096); SCHED; \
    G2_MM1(afc, bfc, ks, 0, 3); if (RD) ARD128(afn[ks][3], _a, 6144); SCHED; \
    G2_MM1(afc, bfc, ks, 1, 0); if (RD) ATR(bfn[ks][0], _b0, (ks) * 4096); SCHED; \
    G2_MM1(afc, bfc, ks, 1, 1); if (RD) ATR(bfn[ks][1], _b0, (ks) * 4096 + 512); SCHED; \
    G2_MM1(afc, bfc, ks, 1, 2); if (RD) ATR(bfn[ks][2], _b1, (ks) * 4096); SCHED; \
    G2_MM1(afc, bfc, ks, 1, 3); if (RD) ATR(bfn[ks][3], _b1, (ks) * 4096 + 512); SCHED; \
    G2_MM1(afc, bfc, ks, 2, 0); if (RD) ATR(bfn[ks][4], _b2, (ks) * 4096); SCHED; \
    G2_MM1(afc, bfc, ks, 2, 1); if (RD) ATR(bfn[ks][5], _b2, (ks) * 4096 + 512); SCHED; \
    G2_MM1(afc, bfc, ks, 2, 2); if (RD) ATR(bfn[ks][6], _b3, (ks) * 4096); SCHED; \
    G2_MM1(afc, bfc, ks, 2, 3); if (RD) ATR(bfn[ks][7], _b3, (ks) * 4096 + 512); SCHED; \
    G2_MM1(afc, bfc, ks, 3, 0); G2_MM1(afc, bfc, ks, 3, 1); G2_MM1(afc, bfc, ks, 3, 2); G2_MM1(afc, bfc, ks, 3, 3); SCHED; }
#define G2_STEP(t, afc, bfc, afn, bfn, RD, PF) { \
    WAIT_L(0); BAR; SCHED; \
    const unsigned _sb = lds0 + (unsigned)(((t) + 1) & 3) * G2_STAGE; \
    const unsigned _b0 = _sb + bO0, _b1 = _sb + bO1, _b2 = _sb + bO2, _b3 = _sb + bO3; \
    G2_HALF(afc, bfc, afn, bfn, _sb, 0, RD); \
    PF; SCHED; \
    G2_HALF(afc, bfc, afn, bfn, _sb, 1, RD); }

  BAR; SCHED;
  STAMPK(1, 1);
  G2_RDK(aX, bX, lds0, 0); G2_RDK(aX, bX, lds0, 1);
  const int nh = isF ? 56 - G2_N0 : G2_N0;
  G2_STEP(0, aX, bX, aY, bY, true, GST4_SC1(odst, ov[0]));
  G2_STEP(1, aY, bY, aX, bX, true, GST4_SC1(odst + 1024, ov[1]));
  G2_STEP(2, aX, bX, aY, bY, true, GST4_SC1(odst + 2048, ov[2]));
  G2_STEP(3, aY, bY, aX, bX, true, GST4_SC1(odst + 3072, ov[3]));
  int t = 4;
  for (; t < 8; t += 2) {
    G2_STEP(t, aX, bX, aY, bY, true, (void)0);
    G2_STEP(t + 1, aY, bY, aX, bX, true, (void)0);
  }
  WAIT_V(0);
  G2_STEP(t, aX, bX, aY, bY, true, (void)0);
  if (threadIdx.x == 0) __hip_atomic_fetch_add((unsigned*)(ws + WS_CNT + (b >> 5) * 256), 1u, __ATOMIC_RELAXED, __HIP_MEMORY_SCOPE_AGENT);
  G2_STEP(t + 1, aY, bY, aX, bX, true, (void)0);
  t += 2;
  for (; t < nh - (isF ? 0 : 2); t += 2) {
    G2_STEP(t, aX, bX, aY, bY, true, (void)0);
    G2_STEP(t + 1, aY, bY, aX, bX, true, (void)0);
  }
  STAMPK(1, 2);
  if (isF) {
#pragma unroll
    for (int m = 0; m < 4; ++m)
#pragma unroll
      for (int j = 0; j < 4; ++j) acc[m][j] *= og[m];
    for (; t < n - 2; t += 2) {
      G2_STEP(t, aX, bX, aY, bY, true, (void)0);
      G2_STEP(t + 1, aY, bY, aX, bX, true, (void)0);
    }
  }
  G2_STEP(t, aX, bX, aY, bY, true, (void)0);
  G2_STEP(t + 1, aY, bY, aX, bX, false, (void)0);
  STAMPK(1, 3);
  if (!isF) {
#pragma unroll
    for (int m = 0; m < 4; ++m)
#pragma unroll
      for (int j = 0; j < 4; ++j) { f32x4 v = acc[m][j] * og[m]; GST4_SC1(part + wave * 16384 + (m * 4 + j) * 1024 + l * 16, v); }
    WAIT_V(0); BAR;
    STAMPK(1, 4);
  } else {
    BAR; SCHED;
    STAMPK(1, 4);
    const unsigned pa = lds0 + (unsigned)((((n - 4) + (wave >> 1)) & 3) * G2_STAGE + (wave & 1) * 16384 + l * 16);
#pragma unroll
    for (int m = 0; m < 4; ++m) {
      f32x4 p0, p1, p2, p3;
      ARD128(p0, pa, (m * 4 + 0) * 1024); ARD128(p1, pa, (m * 4 + 1) * 1024); ARD128(p2, pa, (m * 4 + 2) * 1024); ARD128(p3, pa, (m * 4 + 3) * 1024);
      WAIT_L(0); SCHED;
      f32x4 v0 = acc[m][0] + p0, v1 = acc[m][1] + p1, v2 = acc[m][2] + p2, v3 = acc[m][3] + p3;
      __bf16* dst = out + (size_t)(rt * 128 + wm * 64 + m * 16 + (l & 15)) * D_ + ctile * 128 + wn * 64 + (g4 & 1) * 16 + (g4 >> 1) * 8;
      u32x2 x0 = __builtin_amdgcn_permlane16_swap(pack2(v0[0], v0[1]), pack2(v1[0], v1[1]), false, false);
      u32x2 x1 = __builtin_amdgcn_permlane16_swap(pack2(v0[2], v0[3]), pack2(v1[2], v1[3]), false, false);
      u32x2 y0 = __builtin_amdgcn_permlane16_swap(pack2(v2[0], v2[1]), pack2(v3[0], v3[1]), false, false);
      u32x2 y1 = __builtin_amdgcn_permlane16_swap(pack2(v2[2], v2[3]), pack2(v3[2], v3[3]), false, false);
      *(u32x4*)(dst) = u32x4{x0[0], x1[0], x0[1], x1[1]};
      *(u32x4*)(dst + 32) = u32x4{y0[0], y1[0], y0[1], y1[1]};
    }
  }
  STAMPK(1, 5);
}

extern "C" void kernel_launch(void* const* d_in, const int* in_sizes, int n_in, void* d_out, int out_size, void* d_ws,
                              size_t ws_size, hipStream_t stream) {
  const __bf16* x = (const __bf16*)d_in[0];
  const __bf16* wqkv = (const __bf16*)d_in[1];
  const __bf16* wo = (const __bf16*)d_in[2];
  const __bf16* wg = (const __bf16*)d_in[4];
  const __bf16* wu = (const __bf16*)d_in[5];
  const __bf16* wd = (const __bf16*)d_in[6];
  const __bf16* gw = (const __bf16*)d_in[7];
  hipFuncSetAttribute((const void*)gemm1_kernel, hipFuncAttributeMaxDynamicSharedMemorySize, G1_LDS);
  hipFuncSetAttribute((const void*)gemm2_kernel, hipFuncAttributeMaxDynamicSharedMemorySize, G2_LDS);
  gemm1_kernel<<<256, 512, G1_LDS, stream>>>(x, wqkv, wg, wu, gw, (char*)d_ws, wd, wo);
  gemm2_kernel<<<256, 512, G2_LDS, stream>>>(wo, wd, (char*)d_ws, (__bf16*)d_out);
}
```

```cpp
#include <hip/hip_runtime.h>
#include <hip/hip_bf16.h>
#include <stdint.h>

typedef __attribute__((ext_vector_type(8))) short s16x8;
typedef __attribute__((ext_vector_type(4))) short s16x4;
typedef __attribute__((ext_vector_type(4))) float f32x4;
typedef __attribute__((ext_vector_type(2))) float f32x2;
typedef __attribute__((ext_vector_type(2))) __bf16 bf16x2;
typedef __attribute__((ext_vector_type(2))) unsigned u32x2;
typedef __attribute__((ext_vector_type(4))) unsigned u32x4;
typedef __attribute__((address_space(3))) s16x4* lds_s16x4_p;
typedef __attribute__((address_space(3))) s16x8* lds_s16x8_p;
typedef __attribute__((address_space(3))) char* lds_char_p;

#define S_ 2048
#define D_ 1024
#define F_ 3584
#define AIDX(row, col) ((size_t)(((row) >> 7) * 72 + ((col) >> 6)) * 8192 + (size_t)(((row) & 127) * 64 + ((col) & 63)))
#define PQIDX(row, head, d) ((size_t)(head) * 65536 + (size_t)((row) * 32 + (d)))
#define KA_ 4608

#define WS_ABUF 0ull
#define WS_PQ   (WS_ABUF + (size_t)S_ * KA_ * 2)
#define WS_KVP  (WS_PQ + (size_t)S_ * D_ * 2)
#define WS_ZP   (WS_KVP + (size_t)8 * 32 * 1024 * 4)
#define WS_G    (WS_ZP + (size_t)8 * 32 * 32 * 4)
#define WS_CNT  (WS_G + (size_t)S_ * 4)
#define WS_PART (WS_CNT + 16384)

#define STAMPK(kid, i) do {} while (0)
#define WAIT_V(n) asm volatile("s_waitcnt vmcnt(" #n ")" ::: "memory")
#define WAIT_L(n) asm volatile("s_waitcnt lgkmcnt(" #n ")" ::: "memory")
#define BAR __builtin_amdgcn_s_barrier()
#define SCHED __builtin_amdgcn_sched_barrier(0)

__device__ __forceinline__ float bfr(float v) { return (float)(__bf16)v; }
__device__ __forceinline__ unsigned pack2(float a, float b) {
  f32x2 v = {a, b};
  return __builtin_bit_cast(unsigned, __builtin_convertvector(v, bf16x2));
}
__device__ __forceinline__ f32x2 unpk2(unsigned p) {
  return f32x2{__builtin_bit_cast(float, p << 16), __builtin_bit_cast(float, p & 0xffff0000u)};
}
__device__ __forceinline__ unsigned phi2(float a0, float a1) {
  const f32x2 t = unpk2(pack2(a0, a1));
  const f32x2 p = t + 1.0f, x = t * 1.4426950408889634f;
  const float e0 = __builtin_amdgcn_exp2f(x[0]), e1 = __builtin_amdgcn_exp2f(x[1]);
  const f32x2 r = unpk2(pack2(t[0] > 0.f ? p[0] : e0, t[1] > 0.f ? p[1] : e1)) + 1e-6f;
  return pack2(r[0], r[1]);
}
__device__ __forceinline__ unsigned glu2(float g0, float g1, float u0, float u1) {
  const f32x2 h = unpk2(pack2(g0, g1)) * unpk2(pack2(u0, u1));
  return pack2(h[0], h[1]);
}
__device__ __forceinline__ s16x8 lds_rd128(unsigned addr) { return *(lds_s16x8_p)(size_t)addr; }
__device__ __forceinline__ s16x4 lds_tr(unsigned addr) {
  return __builtin_amdgcn_ds_read_tr16_b64_v4i16((lds_s16x4_p)(size_t)addr);
}
__device__ __forceinline__ s16x8 lds_trfrag(unsigned addr) {
  s16x4 lo = lds_tr(addr), hi = lds_tr(addr + 512);
  return __builtin_shufflevector(lo, hi, 0, 1, 2, 3, 4, 5, 6, 7);
}
#define ARD128(dst, addr, off) asm volatile("ds_read_b128 %0, %1 offset:%2" : "=v"(dst) : "v"(addr), "i"(off) : "memory")
#define ATR(dst, addr, off) asm volatile("ds_read_b64_tr_b16 %0, %1 offset:%2" : "=v"(dst) : "v"(addr), "i"(off) : "memory")
#define ATRF(lo, hi, addr, off) { ATR(lo, addr, off); ATR(hi, addr, (off) + 512); }
#define CAT(lo, hi) __builtin_shufflevector(lo, hi, 0, 1, 2, 3, 4, 5, 6, 7)
__device__ __forceinline__ void glds16(const void* g, unsigned lds_addr) {
  __builtin_amdgcn_global_load_lds((const __attribute__((address_space(1))) unsigned*)g,
                                   (__attribute__((address_space(3))) unsigned*)(size_t)lds_addr, 16, 0, 0);
}
#define MFMA(a, b, c) __builtin_amdgcn_mfma_f32_16x16x32_bf16(a, b, c, 0, 0, 0)

#define G1_ASLOT 32768
#define G1_BBASE 65536
#define G1_BSLOT 20480
#define G1_PFB (G1_BBASE + 4 * G1_BSLOT)
#define G1_LDS (G1_PFB + 1024)
#define G1_PFW0 3

__global__ __launch_bounds__(512, 2) void gemm1_kernel(const __bf16* __restrict__ x, const __bf16* __restrict__ wqkv,
                                                       const __bf16* __restrict__ wg, const __bf16* __restrict__ wu,
                                                       const __bf16* __restrict__ gate_w, char* __restrict__ ws,
                                                       const __bf16* __restrict__ wd, const __bf16* __restrict__ wo) {
  extern __shared__ __attribute__((aligned(16))) char smem[];
  const unsigned lds0 = (unsigned)(size_t)(lds_char_p)smem;
  const int b = blockIdx.x;
  const int xcd = b & 7, idx = b >> 3, rb = idx & 7, ct = xcd * 4 + (idx >> 3);
  const bool isA = ct < 16;
  const int f0 = isA ? ct * 64 : 1024 + (ct - 16) * 160;
  const int wave = __builtin_amdgcn_readfirstlane(threadIdx.x >> 6);
  const int l = threadIdx.x & 63;
  const int grp = wave >> 2, mw = wave & 3;
  STAMPK(0, 0);

  const char* xa = (const char*)x + (size_t)(rb * 256 + mw * 64) * 2048;
  const unsigned voffA = (unsigned)((l >> 3) * 2048 + (((l & 7) ^ (l >> 3)) * 16));
  const int krow = 8 * mw + (l >> 3);
  const int cl = (l & 7) ^ ((((l >> 4) & 1) | ((mw & 1) << 1)) << 1);
  const int rs0 = isA ? 3072 : F_;
  const unsigned voffQ = (unsigned)(krow * rs0 * 2 + cl * 16);
  const unsigned voffF = (unsigned)(krow * F_ * 2 + cl * 16);
  const char* sb0 = isA ? (const char*)(wqkv + ct * 64) : (const char*)(wg + f0);
  const char* sb1 = isA ? (const char*)(wqkv + 1024 + ct * 64) : (const char*)(wu + f0);
  const char* sb2 = isA ? (const char*)(wqkv + 2048 + ct * 64) : (const char*)(wg + f0 + 64);
  const char* sb3 = isA ? (const char*)(wg + f0) : (const char*)(wu + f0 + 64);
  const char* p4 = isA ? (const char*)(wu + f0 + cl * 8)
                       : (cl < 4 ? (const char*)(wg + f0 + 128 + cl * 8) : (const char*)(wu + f0 + 128 + (cl - 4) * 8));
  p4 += (size_t)krow * F_ * 2;
  const size_t pst0 = (size_t)32 * rs0 * 2, pstF = (size_t)32 * F_ * 2;

#define G1_LDA(kt, i) glds16(xa + (size_t)(kt) * 128 + (i) * 16384 + voffA, lds0 + ((kt) & 1) * G1_ASLOT + mw * 8192 + (i) * 1024)
#define G1_BDST(p, s) (lds0 + G1_BBASE + ((p) & 3) * G1_BSLOT + (s) * 4096 + mw * 1024)
#define G1_LDB0(p) glds16(sb0 + (size_t)(p) * pst0 + voffQ, G1_BDST(p, 0))
#define G1_LDB1(p) glds16(sb1 + (size_t)(p) * pst0 + voffQ, G1_BDST(p, 1))
#define G1_LDB2(p) glds16(sb2 + (size_t)(p) * pst0 + voffQ, G1_BDST(p, 2))
#define G1_LDB3(p) glds16(sb3 + (size_t)(p) * pstF + voffF, G1_BDST(p, 3))
#define G1_LDB4(p) glds16(p4 + (size_t)(p) * pstF, G1_BDST(p, 4))

#define G1_PF(sl) { int _l = threadIdx.x & 63; asm volatile("" : "+v"(_l)); \
    const int _s = (sl) + mw, _c = _s > 8 ? 8 : _s; \
    const char* _p = (_c < 7 ? (const char*)wd + (size_t)_c * 1048576 : (const char*)wo + (size_t)(_c - 7) * 1048576) + (size_t)(unsigned)((b * 64 + _l) * 64); \
    __builtin_amdgcn_global_load_lds((const __attribute__((address_space(1))) unsigned*)_p, \
                                     (__attribute__((address_space(3))) unsigned*)(size_t)(lds0 + G1_PFB + mw * 256), 4, 0, 0); }

  const int g4 = l >> 4, q4 = (l & 15) >> 2, pp = l & 3;
  unsigned aAddr0 = lds0 + (unsigned)((mw * 64 + (l & 15)) * 128 + ((g4 ^ (l & 7)) * 16));
  unsigned aAddr1 = aAddr0 ^ 64u;
  const int sh = ((q4 >> 1) & 1) | ((g4 & 1) << 1);
  const unsigned bl = lds0 + G1_BBASE + (unsigned)((8 * g4 + q4) * 128 + (pp >> 1) * 16 + (pp & 1) * 8);
  unsigned bA0 = bl + (unsigned)(((2 * grp) ^ sh) * 32);
  unsigned bA1 = bl + (unsigned)(((2 * grp + 1) ^ sh) * 32);
  unsigned bQ0 = bl + 4 * 4096 + (unsigned)(((isA ? 2 * grp : grp) ^ sh) * 32);
  unsigned bQ1 = bl + 4 * 4096 + (unsigned)(((isA ? 2 * grp + 1 : grp + 2) ^ sh) * 32);
  int bdelta = 2 * G1_BSLOT;

  f32x4 acc[4][10];
#pragma unroll
  for (int m = 0; m < 4; ++m)
#pragma unroll
    for (int j = 0; j < 10; ++j) acc[m][j] = f32x4{0.f, 0.f, 0.f, 0.f};
  s16x8 af[4];
  s16x4 bl_[10], bh_[10];

#define G1_RDA(ad) { ARD128(af[0], ad, 0); ARD128(af[1], ad, 2048); ARD128(af[2], ad, 4096); ARD128(af[3], ad, 6144); }
#define G1_RDB(ps) { ATRF(bl_[0], bh_[0], bA0, (ps) * G1_BSLOT); ATRF(bl_[1], bh_[1], bA1, (ps) * G1_BSLOT); \
    ATRF(bl_[2], bh_[2], bA0, (ps) * G1_BSLOT + 4096); ATRF(bl_[3], bh_[3], bA1, (ps) * G1_BSLOT + 4096); \
    ATRF(bl_[4], bh_[4], bA0, (ps) * G1_BSLOT + 8192); ATRF(bl_[5], bh_[5], bA1, (ps) * G1_BSLOT + 8192); \
    ATRF(bl_[6], bh_[6], bA0, (ps) * G1_BSLOT + 12288); ATRF(bl_[7], bh_[7], bA1, (ps) * G1_BSLOT + 12288); \
    ATRF(bl_[8], bh_[8], bQ0, (ps) * G1_BSLOT); ATRF(bl_[9], bh_[9], bQ1, (ps) * G1_BSLOT); }
#define G1_MMA() { SCHED; __builtin_amdgcn_s_setprio(1); \
    _Pragma("unroll") for (int j = 0; j < 10; ++j) { const s16x8 bfj = CAT(bl_[j], bh_[j]); _Pragma("unroll") for (int m = 0; m < 4; ++m) \
      acc[m][j] = MFMA(bfj, af[m], acc[m][j]); } \
    __builtin_amdgcn_s_setprio(0); SCHED; }
#define G1_LDA8(kt) { G1_LDA(kt, 0); G1_LDA(kt, 1); G1_LDA(kt, 2); G1_LDA(kt, 3); G1_LDA(kt, 4); G1_LDA(kt, 5); G1_LDA(kt, 6); G1_LDA(kt, 7); }
#define G1_LDB5(p) { G1_LDB0(p); G1_LDB1(p); G1_LDB2(p); G1_LDB3(p); G1_LDB4(p); }
#define G1_TOGGLE() { aAddr0 ^= G1_ASLOT; aAddr1 ^= G1_ASLOT; bA0 += bdelta; bA1 += bdelta; bQ0 += bdelta; bQ1 += bdelta; bdelta = -bdelta; }

  auto epilogue = [&](const int grp) __attribute__((always_inline)) {
  SCHED;
  STAMPK(0, 2);
  int tz = threadIdx.x; asm volatile("" : "+v"(tz));
  const int ln = tz & 63, tok = ln & 15, qq = ln >> 4;
  const int row0 = rb * 256 + mw * 64;
  __bf16* Abuf = (__bf16*)(ws + WS_ABUF);
#define G1_H0(m, jg, ju) glu2(acc[m][jg][0], acc[m][jg][1], acc[m][ju][0], acc[m][ju][1])
#define G1_H1(m, jg, ju) glu2(acc[m][jg][2], acc[m][jg][3], acc[m][ju][2], acc[m][ju][3])
#define G1_SWAP16(a0, a1, c0, c1) ({ u32x2 _x = __builtin_amdgcn_permlane16_swap(a0, c0, false, false), _y = __builtin_amdgcn_permlane16_swap(a1, c1, false, false); \
      u32x4{_x[0], _y[0], _x[1], _y[1]}; })
#define G1_STM 0
#define G1_ST(ptr, val) *(u32x4*)(ptr) = (val)
#define G1_HST2(m, jg0, ju0, jg1, ju1, fcol) \
      G1_ST(Abuf + AIDX(row0 + (m) * 16, fcol) + tok * 64 + (qq & 1) * 16 + (qq >> 1) * 8, \
            G1_SWAP16(G1_H0(m, jg0, ju0), G1_H1(m, jg0, ju0), G1_H0(m, jg1, ju1), G1_H1(m, jg1, ju1)))
#define G1_HSTORE2(jg0, ju0, jg1, ju1, fcol) _Pragma("unroll") for (int m = 0; m < 4; ++m) { G1_HST2(m, jg0, ju0, jg1, ju1, fcol); }
#define G1_HSTORE1(jg, ju, fcol) _Pragma("unroll") for (int m = 0; m < 4; m += 2) { \
      G1_ST(Abuf + AIDX(row0 + m * 16, fcol) + ((qq & 1) * 16 + tok) * 64 + (qq >> 1) * 8, \
            G1_SWAP16(G1_H0(m, jg, ju), G1_H1(m, jg, ju), G1_H0(m + 1, jg, ju), G1_H1(m + 1, jg, ju))); }

  if (isA) {
    const int head = 2 * ct + grp;
    __bf16* pq = (__bf16*)(ws + WS_PQ);
    const unsigned kvb = lds0 + (unsigned)(grp * 256 * 136 + (mw * 64 + tok) * 136 + qq * 8);
#pragma unroll
    for (int m = 0; m < 4; ++m) {
      G1_HST2(m, 6, 8, 7, 9, f0 + grp * 32);
      SCHED;
#pragma unroll
      for (int jt = 0; jt < 2; ++jt) {
        u32x2 vk = {phi2(acc[m][2 + jt][0], acc[m][2 + jt][1]), phi2(acc[m][2 + jt][2], acc[m][2 + jt][3])};
        *(__attribute__((address_space(3))) u32x2*)(size_t)(kvb + m * 16 * 136 + jt * 32) = vk;
        u32x2 vv = {pack2(acc[m][4 + jt][0], acc[m][4 + jt][1]), pack2(acc[m][4 + jt][2], acc[m][4 + jt][3])};
        *(__attribute__((address_space(3))) u32x2*)(size_t)(kvb + m * 16 * 136 + 64 + jt * 32) = vv;
      }
      SCHED;
    }
    STAMPK(0, 5);
    WAIT_L(0); BAR; asm volatile("" ::: "memory"); SCHED;
    STAMPK(0, 6);
    const int dt = (wave >> 1) & 1, et = wave & 1;
    const int tq = (ln & 15) >> 2, tp = ln & 3;
    const unsigned rbase = lds0 + (unsigned)(grp * 256 * 136 + (8 * qq + tq) * 136 + tp * 8);
    f32x4 kv = {0.f, 0.f, 0.f, 0.f}, zz = {0.f, 0.f, 0.f, 0.f};
    const short one = 0x3F80;
    const s16x8 ones = {one, one, one, one, one, one, one, one};
#pragma unroll
    for (int s = 0; s < 8; ++s) {
      s16x4 k0 = lds_tr(rbase + s * 32 * 136 + dt * 32), k1 = lds_tr(rbase + s * 32 * 136 + 4 * 136 + dt * 32);
      s16x4 v0 = lds_tr(rbase + s * 32 * 136 + 64 + et * 32), v1 = lds_tr(rbase + s * 32 * 136 + 4 * 136 + 64 + et * 32);
      s16x8 kf = __builtin_shufflevector(k0, k1, 0, 1, 2, 3, 4, 5, 6, 7);
      s16x8 vf = __builtin_shufflevector(v0, v1, 0, 1, 2, 3, 4, 5, 6, 7);
      kv = MFMA(kf, vf, kv);
      zz = MFMA(kf, ones, zz);
    }
    SCHED;
#pragma unroll
    for (int m = 0; m < 4; ++m)
      G1_ST(pq + PQIDX(row0 + m * 16 + tok, head, (qq & 1) * 16 + (qq >> 1) * 8),
            G1_SWAP16(phi2(acc[m][0][0], acc[m][0][1]), phi2(acc[m][0][2], acc[m][0][3]), phi2(acc[m][1][0], acc[m][1][1]), phi2(acc[m][1][2], acc[m][1][3])));
    SCHED;
    float* kvp = (float*)(ws + WS_KVP) + ((size_t)rb * 32 + head) * 1024;
    *(f32x4*)(kvp + (et * 16 + tok) * 32 + dt * 16 + qq * 4) = kv;
    if (et == 0 && tok == 0) *(f32x4*)((float*)(ws + WS_ZP) + ((size_t)rb * 32 + head) * 32 + dt * 16 + qq * 4) = zz;
  } else {
    const int grow = ((ct - 16) * 8 + rb) * 16 + wave * 2 + (ln >> 5);
    G1_HSTORE2(0, 2, 1, 3, f0 + grp * 32);
    SCHED;
    u32x4 gx[4], gw[4];
#pragma unroll
    for (int i = 0; i < 4; ++i) { gx[i] = *(const u32x4*)(x + (size_t)grow * D_ + i * 256 + (ln & 31) * 8); gw[i] = *(const u32x4*)(gate_w + i * 256 + (ln & 31) * 8); }
    SCHED;
    G1_HSTORE2(4, 6, 5, 7, f0 + 64 + grp * 32);
    G1_HSTORE1(8, 9, f0 + 128 + grp * 16);
    SCHED;
    f32x2 s2 = {0.f, 0.f};
#pragma unroll
    for (int i = 0; i < 4; ++i)
#pragma unroll
      for (int j = 0; j < 4; ++j) { const f32x2 pr = unpk2(gx[i][j]) * unpk2(gw[i][j]); s2 += unpk2(pack2(pr[0], pr[1])); }
    float sg = s2[0] + s2[1];
#define G1_DPPADD(ctrl) sg += __builtin_bit_cast(float, __builtin_amdgcn_update_dpp(0, __builtin_bit_cast(int, sg), ctrl, 0xf, 0xf, true))
    G1_DPPADD(0xB1); G1_DPPADD(0x4E); G1_DPPADD(0x141); G1_DPPADD(0x140);
    sg += __shfl_xor(sg, 16);
    if ((ln & 31) == 0) ((float*)(ws + WS_G))[grow] = bfr(1.0f / (1.0f + __expf(-bfr(sg))));
    if (b == 4 && tz < 160) *(unsigned*)(ws + WS_CNT + tz * 64) = 0u;
    STAMPK(0, 5); STAMPK(0, 6);
  }
  STAMPK(0, 7);
  };

  if (grp == 0) {
    G1_LDA8(0);
    WAIT_V(0); BAR; SCHED;
    STAMPK(0, 1);
#define G1_ITER0(i, LA, W1) { \
      G1_RDA(aAddr0); G1_RDB(0); if (LA) G1_LDA8((i) + 1); SCHED; WAIT_L(0); BAR; G1_MMA(); BAR; SCHED; \
      G1_RDA(aAddr1); G1_RDB(1); if (LA && (unsigned)((i) - G1_PFW0) < 3u) { G1_PF(4 * ((i) - G1_PFW0)); } SCHED; WAIT_L(0); BAR; G1_MMA(); W1; BAR; SCHED; \
      G1_TOGGLE(); }
    for (int i = 0; i < 15; ++i) G1_ITER0(i, true, if ((unsigned)(i - G1_PFW0) < 3u) WAIT_V(1); else WAIT_V(0));
    G1_ITER0(15, false, (void)0);
    BAR;
    epilogue(0);
  } else {
    G1_LDB5(0); G1_LDB5(1); G1_LDB5(2);
    WAIT_V(10); BAR; SCHED;
    BAR; SCHED;
#define G1_ITER1(i, L0, L1, W0, W1) { \
      G1_RDA(aAddr0); G1_RDB(0); if (L0) G1_LDB5(2 * (i) + 3); SCHED; WAIT_L(0); W0; BAR; G1_MMA(); BAR; SCHED; \
      G1_RDA(aAddr1); G1_RDB(1); if (L1) G1_LDB5(2 * (i) + 4); SCHED; WAIT_L(0); W1; BAR; G1_MMA(); BAR; SCHED; \
      G1_TOGGLE(); }
    for (int i = 0; i < 14; ++i) G1_ITER1(i, true, true, WAIT_V(10), WAIT_V(10));
    G1_ITER1(14, true, false, WAIT_V(10), WAIT_V(5));
    G1_ITER1(15, false, false, WAIT_V(0), (void)0);
    epilogue(1);
  }
  STAMPK(0, 3);
}

#define G2_N0 32
#define G2_STAGE 32768
#define G2_SCR (4 * G2_STAGE)
#define G2_LDS (G2_SCR + 4096)

__device__ __forceinline__ void glds16_sc1(const void* g, unsigned lds_addr) {
  __builtin_amdgcn_global_load_lds((const __attribute__((address_space(1))) unsigned*)g,
                                   (__attribute__((address_space(3))) unsigned*)(size_t)lds_addr, 16, 0, 16);
}
#define GLD4(dst, ptr) asm volatile("global_load_dwordx4 %0, %1, off" : "=v"(dst) : "v"(ptr) : "memory")
#define GST4_SC1(ptr, val) asm volatile("global_store_dwordx4 %0, %1, off sc1\n\ts_nop 1" ::"v"(ptr), "v"(val) : "memory")

__global__ __launch_bounds__(512, 2) void gemm2_kernel(const __bf16* __restrict__ wo, const __bf16* __restrict__ wd,
                                                       char* ws, __bf16* __restrict__ out) {
  extern __shared__ __attribute__((aligned(16))) char smem[];
  const unsigned lds0 = (unsigned)(size_t)(lds_char_p)smem;
  const int b = blockIdx.x;
  const int xcd = b & 7, idx = b >> 3;
  const bool isF = xcd & 1;
  const int rt = ((xcd >> 1) & 1) * 8 + (idx & 7), ctile = (xcd >> 2) * 4 + (idx >> 3);
  const int tile = rt * 8 + ctile;
  const int wave = __builtin_amdgcn_readfirstlane(threadIdx.x >> 6);
  const int l = threadIdx.x & 63;
  __bf16* Abuf = (__bf16*)(ws + WS_ABUF);
  const int s0 = isF ? G2_N0 : 0, n = isF ? 72 - G2_N0 : G2_N0;
  unsigned* pflag = (unsigned*)(ws + WS_CNT + 2048 + tile * 64);
  char* part = ws + WS_PART + (size_t)tile * 65536;
  STAMPK(1, 0);

  typedef __attribute__((address_space(3))) unsigned* lds_u32_p;
  const lds_u32_p kvcnt = (lds_u32_p)(size_t)(lds0 + G2_SCR + 2560);
  if (threadIdx.x == 0) *kvcnt = 0u;
  WAIT_L(0); BAR; SCHED;
  if (wave >= 4) {
    const int lw = wave - 4;
    const char* asrc = (const char*)Abuf + (size_t)rt * 72 * 16384 + lw * 4096;
    const unsigned voffA = (unsigned)((l >> 3) * 128 + (((l & 7) ^ (l >> 3)) * 16));
    const unsigned voffB0 = (unsigned)((lw * 16 + (l >> 3)) * 2048 + (((l & 7) ^ (((l >> 4) & 1) << 1)) * 16));
    const unsigned voffB1 = (voffB0 ^ 64u) + 8 * 2048;
    const char* bsrc_d = (const char*)(wd + ctile * 128);
    const char* bsrc_o = (const char*)(wo + ctile * 128);
#define G2_LOAD(GL, s, slot) { const unsigned _d = lds0 + (unsigned)(slot) * G2_STAGE; \
    const char* _a = asrc + (size_t)(s) * 16384; \
    const char* _b = (s) < 56 ? bsrc_d + (size_t)(s) * 131072 : bsrc_o + (size_t)((s) - 56) * 131072; \
    GL(_a + voffA, _d + lw * 4096); GL(_a + 1024 + voffA, _d + lw * 4096 + 1024); \
    GL(_a + 2048 + voffA, _d + lw * 4096 + 2048); GL(_a + 3072 + voffA, _d + lw * 4096 + 3072); \
    glds16(_b + voffB0, _d + 16384 + lw * 2048); glds16(_b + voffB1, _d + 16384 + lw * 2048 + 1024); \
    glds16(_b + 128 + voffB0, _d + 24576 + lw * 2048); glds16(_b + 128 + voffB1, _d + 24576 + lw * 2048 + 1024); }
    G2_LOAD(glds16, s0, 0); G2_LOAD(glds16, s0 + 1, 1); G2_LOAD(glds16, s0 + 2, 2); G2_LOAD(glds16, s0 + 3, 3);
    WAIT_V(24); BAR;
    int t = 0;
    const int nh = isF ? 56 - G2_N0 : G2_N0;
    for (; t + 4 < nh && t + 4 < n; ++t) { WAIT_V(16); BAR; G2_LOAD(glds16, s0 + t + 4, t & 3); }
    if (isF) {
      unsigned* cnt = (unsigned*)(ws + WS_CNT + (rt >> 1) * 256);
      while (__hip_atomic_load(cnt, __ATOMIC_RELAXED, __HIP_MEMORY_SCOPE_AGENT) < 32u) __builtin_amdgcn_s_sleep(4);
      for (; t + 4 < n; ++t) { WAIT_V(16); BAR; G2_LOAD(glds16_sc1, s0 + t + 4, t & 3); }
      WAIT_V(16); BAR;
      while (__hip_atomic_load(pflag, __ATOMIC_RELAXED, __HIP_MEMORY_SCOPE_AGENT) == 0u) __builtin_amdgcn_s_sleep(4);
#pragma unroll
      for (int i = 0; i < 8; ++i) glds16_sc1(part + lw * 8192 + i * 1024 + l * 16, lds0 + ((n - 4) & 3) * G2_STAGE + lw * 8192 + i * 1024);
      WAIT_V(16); BAR;
#pragma unroll
      for (int i = 0; i < 8; ++i) glds16_sc1(part + 32768 + lw * 8192 + i * 1024 + l * 16, lds0 + ((n - 3) & 3) * G2_STAGE + lw * 8192 + i * 1024);
      WAIT_V(16); BAR;
      BAR;
      WAIT_V(0); BAR;
    } else {
      WAIT_V(16); BAR;
      WAIT_V(8); BAR;
      WAIT_V(0); BAR;
      BAR;
      BAR;
      if (threadIdx.x == 256) __hip_atomic_store(pflag, 1u, __ATOMIC_RELAXED, __HIP_MEMORY_SCOPE_AGENT);
    }
    return;
  }

  const int wm = wave >> 1, wn = wave & 1;
  const int g4 = l >> 4, q4 = (l & 15) >> 2, pp = l & 3;
  const float* gv = (const float*)(ws + WS_G);
  u32x4 ov[4];
  __bf16* odst;
  {
    const int head = b & 31, r0 = (b >> 5) * 256 + wave * 64;
    const int e = l & 15;
    const float* kvp = (const float*)(ws + WS_KVP) + (size_t)head * 1024 + wave * 256 + l * 4;
    const float* zp = (const float*)(ws + WS_ZP) + (size_t)head * 32 + (l & 7) * 4;
    const __bf16* pq = (const __bf16*)(ws + WS_PQ) + PQIDX(r0 + e, head, g4 * 8);
    f32x4 kp[8], zq[8];
    s16x8 pf[4];
    float gr[4];
#pragma unroll
    for (int r = 0; r < 8; ++r) { kp[r] = *(const f32x4*)(kvp + (size_t)r * 32 * 1024); zq[r] = *(const f32x4*)(zp + (size_t)r * 1024); }
#pragma unroll
    for (int m = 0; m < 4; ++m) { pf[m] = *(const s16x8*)(pq + m * 16 * 32); gr[m] = gv[r0 + m * 16 + e]; }
    SCHED;
    f32x4 ks = ((kp[0] + kp[1]) + (kp[2] + kp[3])) + ((kp[4] + kp[5]) + (kp[6] + kp[7]));
    f32x4 zs = ((zq[0] + zq[1]) + (zq[2] + zq[3])) + ((zq[4] + zq[5]) + (zq[6] + zq[7]));
    *(__attribute__((address_space(3))) u32x2*)(size_t)(lds0 + G2_SCR + wave * 512 + l * 8) = u32x2{pack2(ks[0], ks[1]), pack2(ks[2], ks[3])};
    *(__attribute__((address_space(3))) u32x2*)(size_t)(lds0 + G2_SCR + 2048 + wave * 64 + (l & 7) * 8) = u32x2{pack2(zs[0], zs[1]), pack2(zs[2], zs[3])};
    STAMPK(1, 6);
    WAIT_L(0);
    if (l == 0) __hip_atomic_fetch_add(kvcnt, 1u, __ATOMIC_RELAXED, __HIP_MEMORY_SCOPE_WORKGROUP);
    while (__hip_atomic_load(kvcnt, __ATOMIC_RELAXED, __HIP_MEMORY_SCOPE_WORKGROUP) < 4u) __builtin_amdgcn_s_sleep(1);
    WAIT_L(0); SCHED;
    s16x8 kf0, kf1, zf;
    { const unsigned ka = lds0 + G2_SCR + (unsigned)(e * 64 + g4 * 16), za = lds0 + G2_SCR + 2048 + (unsigned)(wave * 64 + g4 * 16);
      ARD128(kf0, ka, 0); ARD128(kf1, ka, 1024); ARD128(zf, za, 0); WAIT_L(0); SCHED; }
#pragma unroll
    for (int m = 0; m < 4; ++m) {
      f32x4 z4 = {0, 0, 0, 0};
      f32x4 n0 = MFMA(kf0, pf[m], z4), n1 = MFMA(kf1, pf[m], z4), dn = MFMA(zf, pf[m], z4);
      const float rd = __builtin_amdgcn_rcpf(bfr(bfr(dn[0]) + 1e-6f)), g = gr[m];
#define G2_O2(a, c) ({ f32x2 _q = unpk2(pack2(a, c)) * rd; _q = unpk2(pack2(_q[0], _q[1])) * g; pack2(_q[0], _q[1]); })
      unsigned a0 = G2_O2(n0[0], n0[1]), a1 = G2_O2(n0[2], n0[3]), c0 = G2_O2(n1[0], n1[1]), c1 = G2_O2(n1[2], n1[3]);
      u32x2 s0_ = __builtin_amdgcn_permlane16_swap(a0, c0, false, false), s1_ = __builtin_amdgcn_permlane16_swap(a1, c1, false, false);
      ov[m] = u32x4{s0_[0], s1_[0], s0_[1], s1_[1]};
    }
    odst = Abuf + AIDX(r0, F_ + head * 32) + e * 64 + (g4 & 1) * 16 + (g4 >> 1) * 8;
  }
  STAMPK(1, 7);
  float og[4];
#pragma unroll
  for (int m = 0; m < 4; ++m) og[m] = bfr(1.0f - gv[rt * 128 + wm * 64 + m * 16 + (l & 15)]);

  const unsigned aOff0 = (unsigned)((wm * 64 + (l & 15)) * 128 + ((g4 ^ (l & 7)) * 16));
  const int sh = ((q4 >> 1) & 1) | ((g4 & 1) << 1);
  const unsigned bOffL = 16384u + (unsigned)(wn * 8192 + (8 * g4 + q4) * 128 + (pp >> 1) * 16 + (pp & 1) * 8);
  const unsigned bO0 = bOffL + (unsigned)((0 ^ sh) * 32), bO1 = bOffL + (unsigned)((1 ^ sh) * 32);
  const unsigned bO2 = bOffL + (unsigned)((2 ^ sh) * 32), bO3 = bOffL + (unsigned)((3 ^ sh) * 32);

  f32x4 acc[4][4];
#pragma unroll
  for (int m = 0; m < 4; ++m)
#pragma unroll
    for (int j = 0; j < 4; ++j) acc[m][j] = f32x4{0.f, 0.f, 0.f, 0.f};
  u32x4 aX[2][4], aY[2][4];
  u32x2 bX[2][8], bY[2][8];
#define G2_RDK(af, bf, sb, ks) { const unsigned _a = (sb) + (aOff0 ^ ((ks) * 64u)); \
    ARD128(af[ks][0], _a, 0); ARD128(af[ks][1], _a, 2048); ARD128(af[ks][2], _a, 4096); ARD128(af[ks][3], _a, 6144); \
    { const unsigned _b = (sb) + bO0; ATRF(bf[ks][0], bf[ks][1], _b, (ks) * 4096); } { const unsigned _b = (sb) + bO1; ATRF(bf[ks][2], bf[ks][3], _b, (ks) * 4096); } \
    { const unsigned _b = (sb) + bO2; ATRF(bf[ks][4], bf[ks][5], _b, (ks) * 4096); } { const unsigned _b = (sb) + bO3; ATRF(bf[ks][6], bf[ks][7], _b, (ks) * 4096); } }
#define G2_MMK(af, bf, ks) { _Pragma("unroll") for (int j = 0; j < 4; ++j) { \
    const s16x8 _f = __builtin_bit_cast(s16x8, __builtin_shufflevector(bf[ks][2 * j], bf[ks][2 * j + 1], 0, 1, 2, 3)); \
    _Pragma("unroll") for (int m = 0; m < 4; ++m) acc[m][j] = MFMA(_f, __builtin_bit_cast(s16x8, af[ks][m]), acc[m][j]); } }
#define G2_MM1(af, bf, ks, j, m) acc[m][j] = MFMA(__builtin_bit_cast(s16x8, __builtin_shufflevector(bf[ks][2 * (j)], bf[ks][2 * (j) + 1], 0, 1, 2, 3)), \
                                                  __builtin_bit_cast(s16x8, af[ks][m]), acc[m][j])
#define G2_HALF(afc, bfc, afn, bfn, sb, ks, RD) { \
    const unsigned _a = (sb) + (aOff0 ^ ((ks) * 64u)); \
    G2_MM1(afc, bfc, ks, 0, 0); if (RD) ARD128(afn[ks][0], _a, 0); SCHED; \
    G2_MM1(afc, bfc, ks, 0, 1); if (RD) ARD128(afn[ks][1], _a, 2048); SCHED; \
    G2_MM1(afc, bfc, ks, 0, 2); if (RD) ARD128(afn[ks][2], _a, 4096); SCHED; \
    G2_MM1(afc, bfc, ks, 0, 3); if (RD) ARD128(afn[ks][3], _a, 6144); SCHED; \
    G2_MM1(afc, bfc, ks, 1, 0); if (RD) ATR(bfn[ks][0], _b0, (ks) * 4096); SCHED; \
    G2_MM1(afc, bfc, ks, 1, 1); if (RD) ATR(bfn[ks][1], _b0, (ks) * 4096 + 512); SCHED; \
    G2_MM1(afc, bfc, ks, 1, 2); if (RD) ATR(bfn[ks][2], _b1, (ks) * 4096); SCHED; \
    G2_MM1(afc, bfc, ks, 1, 3); if (RD) ATR(bfn[ks][3], _b1, (ks) * 4096 + 512); SCHED; \
    G2_MM1(afc, bfc, ks, 2, 0); if (RD) ATR(bfn[ks][4], _b2, (ks) * 4096); SCHED; \
    G2_MM1(afc, bfc, ks, 2, 1); if (RD) ATR(bfn[ks][5], _b2, (ks) * 4096 + 512); SCHED; \
    G2_MM1(afc, bfc, ks, 2, 2); if (RD) ATR(bfn[ks][6], _b3, (ks) * 4096); SCHED; \
    G2_MM1(afc, bfc, ks, 2, 3); if (RD) ATR(bfn[ks][7], _b3, (ks) * 4096 + 512); SCHED; \
    G2_MM1(afc, bfc, ks, 3, 0); G2_MM1(afc, bfc, ks, 3, 1); G2_MM1(afc, bfc, ks, 3, 2); G2_MM1(afc, bfc, ks, 3, 3); SCHED; }
#define G2_STEP(t, afc, bfc, afn, bfn, RD, PF) { \
    WAIT_L(0); BAR; SCHED; \
    const unsigned _sb = lds0 + (unsigned)(((t) + 1) & 3) * G2_STAGE; \
    const unsigned _b0 = _sb + bO0, _b1 = _sb + bO1, _b2 = _sb + bO2, _b3 = _sb + bO3; \
    G2_HALF(afc, bfc, afn, bfn, _sb, 0, RD); \
    PF; SCHED; \
    G2_HALF(afc, bfc, afn, bfn, _sb, 1, RD); }

  BAR; SCHED;
  STAMPK(1, 1);
  G2_RDK(aX, bX, lds0, 0); G2_RDK(aX, bX, lds0, 1);
  const int nh = isF ? 56 - G2_N0 : G2_N0;
  G2_STEP(0, aX, bX, aY, bY, true, GST4_SC1(odst, ov[0]));
  G2_STEP(1, aY, bY, aX, bX, true, GST4_SC1(odst + 1024, ov[1]));
  G2_STEP(2, aX, bX, aY, bY, true, GST4_SC1(odst + 2048, ov[2]));
  G2_STEP(3, aY, bY, aX, bX, true, GST4_SC1(odst + 3072, ov[3]));
  int t = 4;
  for (; t < 8; t += 2) {
    G2_STEP(t, aX, bX, aY, bY, true, (void)0);
    G2_STEP(t + 1, aY, bY, aX, bX, true, (void)0);
  }
  WAIT_V(0);
  G2_STEP(t, aX, bX, aY, bY, true, (void)0);
  if (threadIdx.x == 0) __hip_atomic_fetch_add((unsigned*)(ws + WS_CNT + (b >> 5) * 256), 1u, __ATOMIC_RELAXED, __HIP_MEMORY_SCOPE_AGENT);
  G2_STEP(t + 1, aY, bY, aX, bX, true, (void)0);
  t += 2;
  for (; t < nh - (isF ? 0 : 2); t += 2) {
    G2_STEP(t, aX, bX, aY, bY, true, (void)0);
    G2_STEP(t + 1, aY, bY, aX, bX, true, (void)0);
  }
  STAMPK(1, 2);
  if (isF) {
#pragma unroll
    for (int m = 0; m < 4; ++m)
#pragma unroll
      for (int j = 0; j < 4; ++j) acc[m][j] *= og[m];
    for (; t < n - 2; t += 2) {
      G2_STEP(t, aX, bX, aY, bY, true, (void)0);
      G2_STEP(t + 1, aY, bY, aX, bX, true, (void)0);
    }
  }
  G2_STEP(t, aX, bX, aY, bY, true, (void)0);
  G2_STEP(t + 1, aY, bY, aX, bX, false, (void)0);
  STAMPK(1, 3);
  if (!isF) {
#pragma unroll
    for (int m = 0; m < 4; ++m)
#pragma unroll
      for (int j = 0; j < 4; ++j) { f32x4 v = acc[m][j] * og[m]; GST4_SC1(part + wave * 16384 + (m * 4 + j) * 1024 + l * 16, v); }
    WAIT_V(0); BAR;
    STAMPK(1, 4);
  } else {
    BAR; SCHED;
    STAMPK(1, 4);
    const unsigned pa = lds0 + (unsigned)((((n - 4) + (wave >> 1)) & 3) * G2_STAGE + (wave & 1) * 16384 + l * 16);
#pragma unroll
    for (int m = 0; m < 4; ++m) {
      f32x4 p0, p1, p2, p3;
      ARD128(p0, pa, (m * 4 + 0) * 1024); ARD128(p1, pa, (m * 4 + 1) * 1024); ARD128(p2, pa, (m * 4 + 2) * 1024); ARD128(p3, pa, (m * 4 + 3) * 1024);
      WAIT_L(0); SCHED;
      f32x4 v0 = acc[m][0] + p0, v1 = acc[m][1] + p1, v2 = acc[m][2] + p2, v3 = acc[m][3] + p3;
      __bf16* dst = out + (size_t)(rt * 128 + wm * 64 + m * 16 + (l & 15)) * D_ + ctile * 128 + wn * 64 + (g4 & 1) * 16 + (g4 >> 1) * 8;
      u32x2 x0 = __builtin_amdgcn_permlane16_swap(pack2(v0[0], v0[1]), pack2(v1[0], v1[1]), false, false);
      u32x2 x1 = __builtin_amdgcn_permlane16_swap(pack2(v0[2], v0[3]), pack2(v1[2], v1[3]), false, false);
      u32x2 y0 = __builtin_amdgcn_permlane16_swap(pack2(v2[0], v2[1]), pack2(v3[0], v3[1]), false, false);
      u32x2 y1 = __builtin_amdgcn_permlane16_swap(pack2(v2[2], v2[3]), pack2(v3[2], v3[3]), false, false);
      *(u32x4*)(dst) = u32x4{x0[0], x1[0], x0[1], x1[1]};
      *(u32x4*)(dst + 32) = u32x4{y0[0], y1[0], y0[1], y1[1]};
    }
  }
  STAMPK(1, 5);
}

extern "C" void kernel_launch(void* const* d_in, const int* in_sizes, int n_in, void* d_out, int out_size, void* d_ws,
                              size_t ws_size, hipStream_t stream) {
  const __bf16* x = (const __bf16*)d_in[0];
  const __bf16* wqkv = (const __bf16*)d_in[1];
  const __bf16* wo = (const __bf16*)d_in[2];
  const __bf16* wg = (const __bf16*)d_in[4];
  const __bf16* wu = (const __bf16*)d_in[5];
  const __bf16* wd = (const __bf16*)d_in[6];
  const __bf16* gw = (const __bf16*)d_in[7];
  hipFuncSetAttribute((const void*)gemm1_kernel, hipFuncAttributeMaxDynamicSharedMemorySize, G1_LDS);
  hipFuncSetAttribute((const void*)gemm2_kernel, hipFuncAttributeMaxDynamicSharedMemorySize, G2_LDS);
  gemm1_kernel<<<256, 512, G1_LDS, stream>>>(x, wqkv, wg, wu, gw, (char*)d_ws, wd, wo);
  gemm2_kernel<<<256, 512, G2_LDS, stream>>>(wo, wd, (char*)d_ws, (__bf16*)d_out);
}
```

```cpp
#include <hip/hip_runtime.h>
#include <hip/hip_bf16.h>
#include <stdint.h>

typedef __attribute__((ext_vector_type(8))) short s16x8;
typedef __attribute__((ext_vector_type(4))) short s16x4;
typedef __attribute__((ext_vector_type(4))) float f32x4;
typedef __attribute__((ext_vector_type(2))) float f32x2;
typedef __attribute__((ext_vector_type(2))) __bf16 bf16x2;
typedef __attribute__((ext_vector_type(2))) unsigned u32x2;
typedef __attribute__((ext_vector_type(4))) unsigned u32x4;
typedef __attribute__((address_space(3))) s16x4* lds_s16x4_p;
typedef __attribute__((address_space(3))) s16x8* lds_s16x8_p;
typedef __attribute__((address_space(3))) char* lds_char_p;

#define S_ 2048
#define D_ 1024
#define F_ 3584
#define AIDX(row, col) ((size_t)(((row) >> 7) * 72 + ((col) >> 6)) * 8192 + (size_t)(((row) & 127) * 64 + ((col) & 63)))
#define PQIDX(row, head, d) ((size_t)(head) * 65536 + (size_t)((row) * 32 + (d)))
#define KA_ 4608

#define WS_ABUF 0ull
#define WS_PQ   (WS_ABUF + (size_t)S_ * KA_ * 2)
#define WS_KVP  (WS_PQ + (size_t)S_ * D_ * 2)
#define WS_ZP   (WS_KVP + (size_t)8 * 32 * 1024 * 4)
#define WS_G    (WS_ZP + (size_t)8 * 32 * 32 * 4)
#define WS_CNT  (WS_G + (size_t)S_ * 4)
#define WS_PART (WS_CNT + 16384)

#define STAMPK(kid, i) do {} while (0)
#define WAIT_V(n) asm volatile("s_waitcnt vmcnt(" #n ")" ::: "memory")
#define WAIT_L(n) asm volatile("s_waitcnt lgkmcnt(" #n ")" ::: "memory")
#define BAR __builtin_amdgcn_s_barrier()
#define SCHED __builtin_amdgcn_sched_barrier(0)

__device__ __forceinline__ float bfr(float v) { return (float)(__bf16)v; }
__device__ __forceinline__ unsigned pack2(float a, float b) {
  f32x2 v = {a, b};
  return __builtin_bit_cast(unsigned, __builtin_convertvector(v, bf16x2));
}
__device__ __forceinline__ f32x2 unpk2(unsigned p) {
  return f32x2{__builtin_bit_cast(float, p << 16), __builtin_bit_cast(float, p & 0xffff0000u)};
}
__device__ __forceinline__ unsigned phi2(float a0, float a1) {
  const f32x2 t = unpk2(pack2(a0, a1));
  const f32x2 p = t + 1.0f, x = t * 1.4426950408889634f;
  const float e0 = __builtin_amdgcn_exp2f(x[0]), e1 = __builtin_amdgcn_exp2f(x[1]);
  const f32x2 r = unpk2(pack2(t[0] > 0.f ? p[0] : e0, t[1] > 0.f ? p[1] : e1)) + 1e-6f;
  return pack2(r[0], r[1]);
}
__device__ __forceinline__ unsigned glu2(float g0, float g1, float u0, float u1) {
  const f32x2 h = unpk2(pack2(g0, g1)) * unpk2(pack2(u0, u1));
  return pack2(h[0], h[1]);
}
__device__ __forceinline__ s16x8 lds_rd128(unsigned addr) { return *(lds_s16x8_p)(size_t)addr; }
__device__ __forceinline__ s16x4 lds_tr(unsigned addr) {
  return __builtin_amdgcn_ds_read_tr16_b64_v4i16((lds_s16x4_p)(size_t)addr);
}
__device__ __forceinline__ s16x8 lds_trfrag(unsigned addr) {
  s16x4 lo = lds_tr(addr), hi = lds_tr(addr + 512);
  return __builtin_shufflevector(lo, hi, 0, 1, 2, 3, 4, 5, 6, 7);
}
#define ARD128(dst, addr, off) asm volatile("ds_read_b128 %0, %1 offset:%2" : "=v"(dst) : "v"(addr), "i"(off) : "memory")
#define ATR(dst, addr, off) asm volatile("ds_read_b64_tr_b16 %0, %1 offset:%2" : "=v"(dst) : "v"(addr), "i"(off) : "memory")
#define ATRF(lo, hi, addr, off) { ATR(lo, addr, off); ATR(hi, addr, (off) + 512); }
#define CAT(lo, hi) __builtin_shufflevector(lo, hi, 0, 1, 2, 3, 4, 5, 6, 7)
__device__ __forceinline__ void glds16(const void* g, unsigned lds_addr) {
  __builtin_amdgcn_global_load_lds((const __attribute__((address_space(1))) unsigned*)g,
                                   (__attribute__((address_space(3))) unsigned*)(size_t)lds_addr, 16, 0, 0);
}
#define MFMA(a, b, c) __builtin_amdgcn_mfma_f32_16x16x32_bf16(a, b, c, 0, 0, 0)

#define G1_ASLOT 32768
#define G1_BBASE 65536
#define G1_BSLOT 20480
#define G1_PFB (G1_BBASE + 4 * G1_BSLOT)
#define G1_LDS (G1_PFB + 1024)
#define G1_PFW0 14

__global__ __launch_bounds__(512, 2) void gemm1_kernel(const __bf16* __restrict__ x, const __bf16* __restrict__ wqkv,
                                                       const __bf16* __restrict__ wg, const __bf16* __restrict__ wu,
                                                       const __bf16* __restrict__ gate_w, char* __restrict__ ws,
                                                       const __bf16* __restrict__ wd, const __bf16* __restrict__ wo) {
  extern __shared__ __attribute__((aligned(16))) char smem[];
  const unsigned lds0 = (unsigned)(size_t)(lds_char_p)smem;
  const int b = blockIdx.x;
  const int xcd = b & 7, idx = b >> 3, rb = idx & 7, ct = xcd * 4 + (idx >> 3);
  const bool isA = ct < 16;
  const int f0 = isA ? ct * 64 : 1024 + (ct - 16) * 160;
  const int wave = __builtin_amdgcn_readfirstlane(threadIdx.x >> 6);
  const int l = threadIdx.x & 63;
  const int grp = wave >> 2, mw = wave & 3;
  STAMPK(0, 0);

  const char* xa = (const char*)x + (size_t)(rb * 256 + mw * 64) * 2048;
  const unsigned voffA = (unsigned)((l >> 3) * 2048 + (((l & 7) ^ (l >> 3)) * 16));
  const int krow = 8 * mw + (l >> 3);
  const int cl = (l & 7) ^ ((((l >> 4) & 1) | ((mw & 1) << 1)) << 1);
  const int rs0 = isA ? 3072 : F_;
  const unsigned voffQ = (unsigned)(krow * rs0 * 2 + cl * 16);
  const unsigned voffF = (unsigned)(krow * F_ * 2 + cl * 16);
  const char* sb0 = isA ? (const char*)(wqkv + ct * 64) : (const char*)(wg + f0);
  const char* sb1 = isA ? (const char*)(wqkv + 1024 + ct * 64) : (const char*)(wu + f0);
  const char* sb2 = isA ? (const char*)(wqkv + 2048 + ct * 64) : (const char*)(wg + f0 + 64);
  const char* sb3 = isA ? (const char*)(wg + f0) : (const char*)(wu + f0 + 64);
  const char* p4 = isA ? (const char*)(wu + f0 + cl * 8)
                       : (cl < 4 ? (const char*)(wg + f0 + 128 + cl * 8) : (const char*)(wu + f0 + 128 + (cl - 4) * 8));
  p4 += (size_t)krow * F_ * 2;
  const size_t pst0 = (size_t)32 * rs0 * 2, pstF = (size_t)32 * F_ * 2;

#define G1_LDA(kt, i) glds16(xa + (size_t)(kt) * 128 + (i) * 16384 + voffA, lds0 + ((kt) & 1) * G1_ASLOT + mw * 8192 + (i) * 1024)
#define G1_BDST(p, s) (lds0 + G1_BBASE + ((p) & 3) * G1_BSLOT + (s) * 4096 + mw * 1024)
#define G1_LDB0(p) glds16(sb0 + (size_t)(p) * pst0 + voffQ, G1_BDST(p, 0))
#define G1_LDB1(p) glds16(sb1 + (size_t)(p) * pst0 + voffQ, G1_BDST(p, 1))
#define G1_LDB2(p) glds16(sb2 + (size_t)(p) * pst0 + voffQ, G1_BDST(p, 2))
#define G1_LDB3(p) glds16(sb3 + (size_t)(p) * pstF + voffF, G1_BDST(p, 3))
#define G1_LDB4(p) glds16(p4 + (size_t)(p) * pstF, G1_BDST(p, 4))

#define G1_PF(k) { int _l = threadIdx.x & ((k) < 2 ? 63 : 15); asm volatile("" : "+v"(_l)); \
    const unsigned _o = (k) < 2 ? (unsigned)(((k) * 65536 + (b * 4 + mw) * 64) * 64) : (unsigned)((131072 + (b * 4 + mw) * 16) * 64); \
    const char* _p = (_o < 7340032u ? (const char*)wd + _o : (const char*)wo + (_o - 7340032u)) + (size_t)(unsigned)(_l * 64); \
    __builtin_amdgcn_global_load_lds((const __attribute__((address_space(1))) unsigned*)_p, \
                                     (__attribute__((address_space(3))) unsigned*)(size_t)(lds0 + G1_PFB + mw * 256), 4, 0, 0); }

  const int g4 = l >> 4, q4 = (l & 15) >> 2, pp = l & 3;
  unsigned aAddr0 = lds0 + (unsigned)((mw * 64 + (l & 15)) * 128 + ((g4 ^ (l & 7)) * 16));
  unsigned aAddr1 = aAddr0 ^ 64u;
  const int sh = ((q4 >> 1) & 1) | ((g4 & 1) << 1);
  const unsigned bl = lds0 + G1_BBASE + (unsigned)((8 * g4 + q4) * 128 + (pp >> 1) * 16 + (pp & 1) * 8);
  unsigned bA0 = bl + (unsigned)(((2 * grp) ^ sh) * 32);
  unsigned bA1 = bl + (unsigned)(((2 * grp + 1) ^ sh) * 32);
  unsigned bQ0 = bl + 4 * 4096 + (unsigned)(((isA ? 2 * grp : grp) ^ sh) * 32);
  unsigned bQ1 = bl + 4 * 4096 + (unsigned)(((isA ? 2 * grp + 1 : grp + 2) ^ sh) * 32);
  int bdelta = 2 * G1_BSLOT;

  f32x4 acc[4][10];
#pragma unroll
  for (int m = 0; m < 4; ++m)
#pragma unroll
    for (int j = 0; j < 10; ++j) acc[m][j] = f32x4{0.f, 0.f, 0.f, 0.f};
  s16x8 af[4];
  s16x4 bl_[10], bh_[10];

#define G1_RDA(ad) { ARD128(af[0], ad, 0); ARD128(af[1], ad, 2048); ARD128(af[2], ad, 4096); ARD128(af[3], ad, 6144); }
#define G1_RDB(ps) { ATRF(bl_[0], bh_[0], bA0, (ps) * G1_BSLOT); ATRF(bl_[1], bh_[1], bA1, (ps) * G1_BSLOT); \
    ATRF(bl_[2], bh_[2], bA0, (ps) * G1_BSLOT + 4096); ATRF(bl_[3], bh_[3], bA1, (ps) * G1_BSLOT + 4096); \
    ATRF(bl_[4], bh_[4], bA0, (ps) * G1_BSLOT + 8192); ATRF(bl_[5], bh_[5], bA1, (ps) * G1_BSLOT + 8192); \
    ATRF(bl_[6], bh_[6], bA0, (ps) * G1_BSLOT + 12288); ATRF(bl_[7], bh_[7], bA1, (ps) * G1_BSLOT + 12288); \
    ATRF(bl_[8], bh_[8], bQ0, (ps) * G1_BSLOT); ATRF(bl_[9], bh_[9], bQ1, (ps) * G1_BSLOT); }
#define G1_MMA(LD) { SCHED; __builtin_amdgcn_s_setprio(1); \
    _Pragma("unroll") for (int j = 0; j < 10; ++j) { const s16x8 bfj = CAT(bl_[j], bh_[j]); _Pragma("unroll") for (int m = 0; m < 4; ++m) \
      acc[m][j] = MFMA(bfj, af[m], acc[m][j]); SCHED; LD; SCHED; } \
    __builtin_amdgcn_s_setprio(0); SCHED; }
#define G1_LDBJ(p) { if (j == 0) G1_LDB0(p); if (j == 2) G1_LDB1(p); if (j == 4) G1_LDB2(p); if (j == 6) G1_LDB3(p); if (j == 8) G1_LDB4(p); }
#define G1_LDA8(kt) { G1_LDA(kt, 0); G1_LDA(kt, 1); G1_LDA(kt, 2); G1_LDA(kt, 3); G1_LDA(kt, 4); G1_LDA(kt, 5); G1_LDA(kt, 6); G1_LDA(kt, 7); }
#define G1_LDB5(p) { G1_LDB0(p); G1_LDB1(p); G1_LDB2(p); G1_LDB3(p); G1_LDB4(p); }
#define G1_TOGGLE() { aAddr0 ^= G1_ASLOT; aAddr1 ^= G1_ASLOT; bA0 += bdelta; bA1 += bdelta; bQ0 += bdelta; bQ1 += bdelta; bdelta = -bdelta; }

  auto epilogue = [&](const int grp) __attribute__((always_inline)) {
  SCHED;
  STAMPK(0, 2);
  int tz = threadIdx.x; asm volatile("" : "+v"(tz));
  const int ln = tz & 63, tok = ln & 15, qq = ln >> 4;
  const int row0 = rb * 256 + mw * 64;
  __bf16* Abuf = (__bf16*)(ws + WS_ABUF);
#define G1_H0(m, jg, ju) glu2(acc[m][jg][0], acc[m][jg][1], acc[m][ju][0], acc[m][ju][1])
#define G1_H1(m, jg, ju) glu2(acc[m][jg][2], acc[m][jg][3], acc[m][ju][2], acc[m][ju][3])
#define G1_SWAP16(a0, a1, c0, c1) ({ u32x2 _x = __builtin_amdgcn_permlane16_swap(a0, c0, false, false), _y = __builtin_amdgcn_permlane16_swap(a1, c1, false, false); \
      u32x4{_x[0], _y[0], _x[1], _y[1]}; })
#define G1_STM 0
#define G1_ST(ptr, val) *(u32x4*)(ptr) = (val)
#define G1_HST2(m, jg0, ju0, jg1, ju1, fcol) \
      G1_ST(Abuf + AIDX(row0 + (m) * 16, fcol) + tok * 64 + (qq & 1) * 16 + (qq >> 1) * 8, \
            G1_SWAP16(G1_H0(m, jg0, ju0), G1_H1(m, jg0, ju0), G1_H0(m, jg1, ju1), G1_H1(m, jg1, ju1)))
#define G1_HSTORE2(jg0, ju0, jg1, ju1, fcol) _Pragma("unroll") for (int m = 0; m < 4; ++m) { G1_HST2(m, jg0, ju0, jg1, ju1, fcol); }
#define G1_HSTORE1(jg, ju, fcol) _Pragma("unroll") for (int m = 0; m < 4; m += 2) { \
      G1_ST(Abuf + AIDX(row0 + m * 16, fcol) + ((qq & 1) * 16 + tok) * 64 + (qq >> 1) * 8, \
            G1_SWAP16(G1_H0(m, jg, ju), G1_H1(m, jg, ju), G1_H0(m + 1, jg, ju), G1_H1(m + 1, jg, ju))); }

  if (isA) {
    const int head = 2 * ct + grp;
    __bf16* pq = (__bf16*)(ws + WS_PQ);
    const unsigned kvb = lds0 + (unsigned)(grp * 256 * 136 + (mw * 64 + tok) * 136 + qq * 8);
#pragma unroll
    for (int m = 0; m < 4; ++m) {
      G1_HST2(m, 6, 8, 7, 9, f0 + grp * 32);
      SCHED;
#pragma unroll
      for (int jt = 0; jt < 2; ++jt) {
        u32x2 vk = {phi2(acc[m][2 + jt][0], acc[m][2 + jt][1]), phi2(acc[m][2 + jt][2], acc[m][2 + jt][3])};
        *(__attribute__((address_space(3))) u32x2*)(size_t)(kvb + m * 16 * 136 + jt * 32) = vk;
        u32x2 vv = {pack2(acc[m][4 + jt][0], acc[m][4 + jt][1]), pack2(acc[m][4 + jt][2], acc[m][4 + jt][3])};
        *(__attribute__((address_space(3))) u32x2*)(size_t)(kvb + m * 16 * 136 + 64 + jt * 32) = vv;
      }
      SCHED;
    }
    STAMPK(0, 5);
    WAIT_L(0); BAR; asm volatile("" ::: "memory"); SCHED;
    STAMPK(0, 6);
    const int dt = (wave >> 1) & 1, et = wave & 1;
    const int tq = (ln & 15) >> 2, tp = ln & 3;
    const unsigned rbase = lds0 + (unsigned)(grp * 256 * 136 + (8 * qq + tq) * 136 + tp * 8);
    f32x4 kv = {0.f, 0.f, 0.f, 0.f}, zz = {0.f, 0.f, 0.f, 0.f};
    const short one = 0x3F80;
    const s16x8 ones = {one, one, one, one, one, one, one, one};
#pragma unroll
    for (int s = 0; s < 8; ++s) {
      s16x4 k0 = lds_tr(rbase + s * 32 * 136 + dt * 32), k1 = lds_tr(rbase + s * 32 * 136 + 4 * 136 + dt * 32);
      s16x4 v0 = lds_tr(rbase + s * 32 * 136 + 64 + et * 32), v1 = lds_tr(rbase + s * 32 * 136 + 4 * 136 + 64 + et * 32);
      s16x8 kf = __builtin_shufflevector(k0, k1, 0, 1, 2, 3, 4, 5, 6, 7);
      s16x8 vf = __builtin_shufflevector(v0, v1, 0, 1, 2, 3, 4, 5, 6, 7);
      kv = MFMA(kf, vf, kv);
      zz = MFMA(kf, ones, zz);
    }
    SCHED;
#pragma unroll
    for (int m = 0; m < 4; ++m)
      G1_ST(pq + PQIDX(row0 + m * 16 + tok, head, (qq & 1) * 16 + (qq >> 1) * 8),
            G1_SWAP16(phi2(acc[m][0][0], acc[m][0][1]), phi2(acc[m][0][2], acc[m][0][3]), phi2(acc[m][1][0], acc[m][1][1]), phi2(acc[m][1][2], acc[m][1][3])));
    SCHED;
    float* kvp = (float*)(ws + WS_KVP) + ((size_t)rb * 32 + head) * 1024;
    *(f32x4*)(kvp + (et * 16 + tok) * 32 + dt * 16 + qq * 4) = kv;
    if (et == 0 && tok == 0) *(f32x4*)((float*)(ws + WS_ZP) + ((size_t)rb * 32 + head) * 32 + dt * 16 + qq * 4) = zz;
  } else {
    const int grow = ((ct - 16) * 8 + rb) * 16 + wave * 2 + (ln >> 5);
    G1_HSTORE2(0, 2, 1, 3, f0 + grp * 32);
    SCHED;
    u32x4 gx[4], gw[4];
#pragma unroll
    for (int i = 0; i < 4; ++i) { gx[i] = *(const u32x4*)(x + (size_t)grow * D_ + i * 256 + (ln & 31) * 8); gw[i] = *(const u32x4*)(gate_w + i * 256 + (ln & 31) * 8); }
    SCHED;
    G1_HSTORE2(4, 6, 5, 7, f0 + 64 + grp * 32);
    G1_HSTORE1(8, 9, f0 + 128 + grp * 16);
    SCHED;
    f32x2 s2 = {0.f, 0.f};
#pragma unroll
    for (int i = 0; i < 4; ++i)
#pragma unroll
      for (int j = 0; j < 4; ++j) { const f32x2 pr = unpk2(gx[i][j]) * unpk2(gw[i][j]); s2 += unpk2(pack2(pr[0], pr[1])); }
    float sg = s2[0] + s2[1];
#define G1_DPPADD(ctrl) sg += __builtin_bit_cast(float, __builtin_amdgcn_update_dpp(0, __builtin_bit_cast(int, sg), ctrl, 0xf, 0xf, true))
    G1_DPPADD(0xB1); G1_DPPADD(0x4E); G1_DPPADD(0x141); G1_DPPADD(0x140);
    sg += __shfl_xor(sg, 16);
    if ((ln & 31) == 0) ((float*)(ws + WS_G))[grow] = bfr(1.0f / (1.0f + __expf(-bfr(sg))));
    if (b == 4 && tz < 160) *(unsigned*)(ws + WS_CNT + tz * 64) = 0u;
    STAMPK(0, 5); STAMPK(0, 6);
  }
  STAMPK(0, 7);
  };

  if (grp == 0) {
    G1_LDB5(0); G1_LDB5(1); G1_LDB5(2);
    WAIT_V(10); BAR; SCHED;
    STAMPK(0, 1);
#define G1_ITER0(i, L0, L1, W0, W1) { \
      G1_RDA(aAddr0); G1_RDB(0); SCHED; WAIT_L(0); BAR; G1_MMA(if (L0) G1_LDBJ(2 * (i) + 3)); W0; BAR; SCHED; \
      G1_RDA(aAddr1); G1_RDB(1); SCHED; WAIT_L(0); BAR; G1_MMA(if (L1) G1_LDBJ(2 * (i) + 4)); W1; BAR; SCHED; \
      G1_TOGGLE(); }
    for (int i = 0; i < 14; ++i) G1_ITER0(i, true, true, WAIT_V(10), WAIT_V(10));
    G1_ITER0(14, true, false, WAIT_V(10), WAIT_V(5));
    G1_ITER0(15, false, false, WAIT_V(0), (void)0);
    BAR;
    epilogue(0);
  } else {
    G1_LDA8(0); G1_LDA8(1);
    WAIT_V(8); BAR; SCHED;
    BAR; SCHED;
#define G1_PFJ { if (_pw) { if (j == 1) G1_PF(0); if (j == 4) G1_PF(1); if (j == 7) G1_PF(2); } }
#define G1_ITER1(i, LA, W1) { const bool _pw = (i) == G1_PFW0; \
      G1_RDA(aAddr0); G1_RDB(0); SCHED; WAIT_L(0); BAR; G1_MMA(G1_PFJ); BAR; SCHED; \
      G1_RDA(aAddr1); G1_RDB(1); SCHED; WAIT_L(0); W1; BAR; G1_MMA(if (LA && j < 8) G1_LDA((i) + 2, j)); BAR; SCHED; \
      G1_TOGGLE(); }
    for (int i = 0; i < 14; ++i) G1_ITER1(i, true, if (_pw) WAIT_V(3); else WAIT_V(0));
    G1_ITER1(14, false, if (_pw) WAIT_V(3); else WAIT_V(0));
    G1_ITER1(15, false, (void)0);
    epilogue(1);
    WAIT_V(0);
  }
  STAMPK(0, 3);
}

#define G2_N0 32
#define G2_STAGE 32768
#define G2_SCR (4 * G2_STAGE)
#define G2_LDS (G2_SCR + 4096)

__device__ __forceinline__ void glds16_sc1(const void* g, unsigned lds_addr) {
  __builtin_amdgcn_global_load_lds((const __attribute__((address_space(1))) unsigned*)g,
                                   (__attribute__((address_space(3))) unsigned*)(size_t)lds_addr, 16, 0, 16);
}
#define GLD4(dst, ptr) asm volatile("global_load_dwordx4 %0, %1, off" : "=v"(dst) : "v"(ptr) : "memory")
#define GST4_SC1(ptr, val) asm volatile("global_store_dwordx4 %0, %1, off sc1\n\ts_nop 1" ::"v"(ptr), "v"(val) : "memory")

__global__ __launch_bounds__(512, 2) void gemm2_kernel(const __bf16* __restrict__ wo, const __bf16* __restrict__ wd,
                                                       char* ws, __bf16* __restrict__ out) {
  extern __shared__ __attribute__((aligned(16))) char smem[];
  const unsigned lds0 = (unsigned)(size_t)(lds_char_p)smem;
  const int b = blockIdx.x;
  const int xcd = b & 7, idx = b >> 3;
  const bool isF = xcd & 1;
  const int rt = ((xcd >> 1) & 1) * 8 + (idx & 7), ctile = (xcd >> 2) * 4 + (idx >> 3);
  const int tile = rt * 8 + ctile;
  const int wave = __builtin_amdgcn_readfirstlane(threadIdx.x >> 6);
  const int l = threadIdx.x & 63;
  __bf16* Abuf = (__bf16*)(ws + WS_ABUF);
  const int s0 = isF ? G2_N0 : 0, n = isF ? 72 - G2_N0 : G2_N0;
  unsigned* pflag = (unsigned*)(ws + WS_CNT + 2048 + tile * 64);
  char* part = ws + WS_PART + (size_t)tile * 65536;
  STAMPK(1, 0);

  typedef __attribute__((address_space(3))) unsigned* lds_u32_p;
  const lds_u32_p kvcnt = (lds_u32_p)(size_t)(lds0 + G2_SCR + 2560);
  if (threadIdx.x == 0) *kvcnt = 0u;
  WAIT_L(0); BAR; SCHED;
  if (wave >= 4) {
    const int lw = wave - 4;
    const char* asrc = (const char*)Abuf + (size_t)rt * 72 * 16384 + lw * 4096;
    const unsigned voffA = (unsigned)((l >> 3) * 128 + (((l & 7) ^ (l >> 3)) * 16));
    const unsigned voffB0 = (unsigned)((lw * 16 + (l >> 3)) * 2048 + (((l & 7) ^ (((l >> 4) & 1) << 1)) * 16));
    const unsigned voffB1 = (voffB0 ^ 64u) + 8 * 2048;
    const char* bsrc_d = (const char*)(wd + ctile * 128);
    const char* bsrc_o = (const char*)(wo + ctile * 128);
#define G2_LOAD(GL, s, slot) { const unsigned _d = lds0 + (unsigned)(slot) * G2_STAGE; \
    const char* _a = asrc + (size_t)(s) * 16384; \
    const char* _b = (s) < 56 ? bsrc_d + (size_t)(s) * 131072 : bsrc_o + (size_t)((s) - 56) * 131072; \
    GL(_a + voffA, _d + lw * 4096); GL(_a + 1024 + voffA, _d + lw * 4096 + 1024); \
    GL(_a + 2048 + voffA, _d + lw * 4096 + 2048); GL(_a + 3072 + voffA, _d + lw * 4096 + 3072); \
    glds16(_b + voffB0, _d + 16384 + lw * 2048); glds16(_b + voffB1, _d + 16384 + lw * 2048 + 1024); \
    glds16(_b + 128 + voffB0, _d + 24576 + lw * 2048); glds16(_b + 128 + voffB1, _d + 24576 + lw * 2048 + 1024); }
    G2_LOAD(glds16, s0, 0); G2_LOAD(glds16, s0 + 1, 1); G2_LOAD(glds16, s0 + 2, 2); G2_LOAD(glds16, s0 + 3, 3);
    WAIT_V(24); BAR;
    int t = 0;
    const int nh = isF ? 56 - G2_N0 : G2_N0;
    for (; t + 4 < nh && t + 4 < n; ++t) { WAIT_V(16); BAR; G2_LOAD(glds16, s0 + t + 4, t & 3); }
    if (isF) {
      unsigned* cnt = (unsigned*)(ws + WS_CNT + (rt >> 1) * 256);
      while (__hip_atomic_load(cnt, __ATOMIC_RELAXED, __HIP_MEMORY_SCOPE_AGENT) < 32u) __builtin_amdgcn_s_sleep(4);
      for (; t + 4 < n; ++t) { WAIT_V(16); BAR; G2_LOAD(glds16_sc1, s0 + t + 4, t & 3); }
      WAIT_V(16); BAR;
      while (__hip_atomic_load(pflag, __ATOMIC_RELAXED, __HIP_MEMORY_SCOPE_AGENT) == 0u) __builtin_amdgcn_s_sleep(4);
#pragma unroll
      for (int i = 0; i < 8; ++i) glds16_sc1(part + lw * 8192 + i * 1024 + l * 16, lds0 + ((n - 4) & 3) * G2_STAGE + lw * 8192 + i * 1024);
      WAIT_V(16); BAR;
#pragma unroll
      for (int i = 0; i < 8; ++i) glds16_sc1(part + 32768 + lw * 8192 + i * 1024 + l * 16, lds0 + ((n - 3) & 3) * G2_STAGE + lw * 8192 + i * 1024);
      WAIT_V(16); BAR;
      BAR;
      WAIT_V(0); BAR;
    } else {
      WAIT_V(16); BAR;
      WAIT_V(8); BAR;
      WAIT_V(0); BAR;
      BAR;
      BAR;
      if (threadIdx.x == 256) __hip_atomic_store(pflag, 1u, __ATOMIC_RELAXED, __HIP_MEMORY_SCOPE_AGENT);
    }
    return;
  }

  const int wm = wave >> 1, wn = wave & 1;
  const int g4 = l >> 4, q4 = (l & 15) >> 2, pp = l & 3;
  const float* gv = (const float*)(ws + WS_G);
  u32x4 ov[4];
  __bf16* odst;
  {
    const int head = b & 31, r0 = (b >> 5) * 256 + wave * 64;
    const int e = l & 15;
    const float* kvp = (const float*)(ws + WS_KVP) + (size_t)head * 1024 + wave * 256 + l * 4;
    const float* zp = (const float*)(ws + WS_ZP) + (size_t)head * 32 + (l & 7) * 4;
    const __bf16* pq = (const __bf16*)(ws + WS_PQ) + PQIDX(r0 + e, head, g4 * 8);
    f32x4 kp[8], zq[8];
    s16x8 pf[4];
    float gr[4];
#pragma unroll
    for (int r = 0; r < 8; ++r) { kp[r] = *(const f32x4*)(kvp + (size_t)r * 32 * 1024); zq[r] = *(const f32x4*)(zp + (size_t)r * 1024); }
#pragma unroll
    for (int m = 0; m < 4; ++m) { pf[m] = *(const s16x8*)(pq + m * 16 * 32); gr[m] = gv[r0 + m * 16 + e]; }
    SCHED;
    f32x4 ks = ((kp[0] + kp[1]) + (kp[2] + kp[3])) + ((kp[4] + kp[5]) + (kp[6] + kp[7]));
    f32x4 zs = ((zq[0] + zq[1]) + (zq[2] + zq[3])) + ((zq[4] + zq[5]) + (zq[6] + zq[7]));
    *(__attribute__((address_space(3))) u32x2*)(size_t)(lds0 + G2_SCR + wave * 512 + l * 8) = u32x2{pack2(ks[0], ks[1]), pack2(ks[2], ks[3])};
    *(__attribute__((address_space(3))) u32x2*)(size_t)(lds0 + G2_SCR + 2048 + wave * 64 + (l & 7) * 8) = u32x2{pack2(zs[0], zs[1]), pack2(zs[2], zs[3])};
    STAMPK(1, 6);
    WAIT_L(0);
    if (l == 0) __hip_atomic_fetch_add(kvcnt, 1u, __ATOMIC_RELAXED, __HIP_MEMORY_SCOPE_WORKGROUP);
    while (__hip_atomic_load(kvcnt, __ATOMIC_RELAXED, __HIP_MEMORY_SCOPE_WORKGROUP) < 4u) __builtin_amdgcn_s_sleep(1);
    WAIT_L(0); SCHED;
    s16x8 kf0, kf1, zf;
    { const unsigned ka = lds0 + G2_SCR + (unsigned)(e * 64 + g4 * 16), za = lds0 + G2_SCR + 2048 + (unsigned)(wave * 64 + g4 * 16);
      ARD128(kf0, ka, 0); ARD128(kf1, ka, 1024); ARD128(zf, za, 0); WAIT_L(0); SCHED; }
#pragma unroll
    for (int m = 0; m < 4; ++m) {
      f32x4 z4 = {0, 0, 0, 0};
      f32x4 n0 = MFMA(kf0, pf[m], z4), n1 = MFMA(kf1, pf[m], z4), dn = MFMA(zf, pf[m], z4);
      const float rd = __builtin_amdgcn_rcpf(bfr(bfr(dn[0]) + 1e-6f)), g = gr[m];
#define G2_O2(a, c) ({ f32x2 _q = unpk2(pack2(a, c)) * rd; _q = unpk2(pack2(_q[0], _q[1])) * g; pack2(_q[0], _q[1]); })
      unsigned a0 = G2_O2(n0[0], n0[1]), a1 = G2_O2(n0[2], n0[3]), c0 = G2_O2(n1[0], n1[1]), c1 = G2_O2(n1[2], n1[3]);
      u32x2 s0_ = __builtin_amdgcn_permlane16_swap(a0, c0, false, false), s1_ = __builtin_amdgcn_permlane16_swap(a1, c1, false, false);
      ov[m] = u32x4{s0_[0], s1_[0], s0_[1], s1_[1]};
    }
    odst = Abuf + AIDX(r0, F_ + head * 32) + e * 64 + (g4 & 1) * 16 + (g4 >> 1) * 8;
  }
  STAMPK(1, 7);
  float og[4];
#pragma unroll
  for (int m = 0; m < 4; ++m) og[m] = bfr(1.0f - gv[rt * 128 + wm * 64 + m * 16 + (l & 15)]);

  const unsigned aOff0 = (unsigned)((wm * 64 + (l & 15)) * 128 + ((g4 ^ (l & 7)) * 16));
  const int sh = ((q4 >> 1) & 1) | ((g4 & 1) << 1);
  const unsigned bOffL = 16384u + (unsigned)(wn * 8192 + (8 * g4 + q4) * 128 + (pp >> 1) * 16 + (pp & 1) * 8);
  const unsigned bO0 = bOffL + (unsigned)((0 ^ sh) * 32), bO1 = bOffL + (unsigned)((1 ^ sh) * 32);
  const unsigned bO2 = bOffL + (unsigned)((2 ^ sh) * 32), bO3 = bOffL + (unsigned)((3 ^ sh) * 32);

  f32x4 acc[4][4];
#pragma unroll
  for (int m = 0; m < 4; ++m)
#pragma unroll
    for (int j = 0; j < 4; ++j) acc[m][j] = f32x4{0.f, 0.f, 0.f, 0.f};
  u32x4 aX[2][4], aY[2][4];
  u32x2 bX[2][8], bY[2][8];
#define G2_RDK(af, bf, sb, ks) { const unsigned _a = (sb) + (aOff0 ^ ((ks) * 64u)); \
    ARD128(af[ks][0], _a, 0); ARD128(af[ks][1], _a, 2048); ARD128(af[ks][2], _a, 4096); ARD128(af[ks][3], _a, 6144); \
    { const unsigned _b = (sb) + bO0; ATRF(bf[ks][0], bf[ks][1], _b, (ks) * 4096); } { const unsigned _b = (sb) + bO1; ATRF(bf[ks][2], bf[ks][3], _b, (ks) * 4096); } \
    { const unsigned _b = (sb) + bO2; ATRF(bf[ks][4], bf[ks][5], _b, (ks) * 4096); } { const unsigned _b = (sb) + bO3; ATRF(bf[ks][6], bf[ks][7], _b, (ks) * 4096); } }
#define G2_MMK(af, bf, ks) { _Pragma("unroll") for (int j = 0; j < 4; ++j) { \
    const s16x8 _f = __builtin_bit_cast(s16x8, __builtin_shufflevector(bf[ks][2 * j], bf[ks][2 * j + 1], 0, 1, 2, 3)); \
    _Pragma("unroll") for (int m = 0; m < 4; ++m) acc[m][j] = MFMA(_f, __builtin_bit_cast(s16x8, af[ks][m]), acc[m][j]); } }
#define G2_MM1(af, bf, ks, j, m) acc[m][j] = MFMA(__builtin_bit_cast(s16x8, __builtin_shufflevector(bf[ks][2 * (j)], bf[ks][2 * (j) + 1], 0, 1, 2, 3)), \
                                                  __builtin_bit_cast(s16x8, af[ks][m]), acc[m][j])
#define G2_HALF(afc, bfc, afn, bfn, sb, ks, RD) { \
    const unsigned _a = (sb) + (aOff0 ^ ((ks) * 64u)); \
    G2_MM1(afc, bfc, ks, 0, 0); if (RD) ARD128(afn[ks][0], _a, 0); SCHED; \
    G2_MM1(afc, bfc, ks, 0, 1); if (RD) ARD128(afn[ks][1], _a, 2048); SCHED; \
    G2_MM1(afc, bfc, ks, 0, 2); if (RD) ARD128(afn[ks][2], _a, 4096); SCHED; \
    G2_MM1(afc, bfc, ks, 0, 3); if (RD) ARD128(afn[ks][3], _a, 6144); SCHED; \
    G2_MM1(afc, bfc, ks, 1, 0); if (RD) ATR(bfn[ks][0], _b0, (ks) * 4096); SCHED; \
    G2_MM1(afc, bfc, ks, 1, 1); if (RD) ATR(bfn[ks][1], _b0, (ks) * 4096 + 512); SCHED; \
    G2_MM1(afc, bfc, ks, 1, 2); if (RD) ATR(bfn[ks][2], _b1, (ks) * 4096); SCHED; \
    G2_MM1(afc, bfc, ks, 1, 3); if (RD) ATR(bfn[ks][3], _b1, (ks) * 4096 + 512); SCHED; \
    G2_MM1(afc, bfc, ks, 2, 0); if (RD) ATR(bfn[ks][4], _b2, (ks) * 4096); SCHED; \
    G2_MM1(afc, bfc, ks, 2, 1); if (RD) ATR(bfn[ks][5], _b2, (ks) * 4096 + 512); SCHED; \
    G2_MM1(afc, bfc, ks, 2, 2); if (RD) ATR(bfn[ks][6], _b3, (ks) * 4096); SCHED; \
    G2_MM1(afc, bfc, ks, 2, 3); if (RD) ATR(bfn[ks][7], _b3, (ks) * 4096 + 512); SCHED; \
    G2_MM1(afc, bfc, ks, 3, 0); G2_MM1(afc, bfc, ks, 3, 1); G2_MM1(afc, bfc, ks, 3, 2); G2_MM1(afc, bfc, ks, 3, 3); SCHED; }
#define G2_STEP(t, afc, bfc, afn, bfn, RD, PF) { \
    WAIT_L(0); BAR; SCHED; \
    const unsigned _sb = lds0 + (unsigned)(((t) + 1) & 3) * G2_STAGE; \
    const unsigned _b0 = _sb + bO0, _b1 = _sb + bO1, _b2 = _sb + bO2, _b3 = _sb + bO3; \
    G2_HALF(afc, bfc, afn, bfn, _sb, 0, RD); \
    PF; SCHED; \
    G2_HALF(afc, bfc, afn, bfn, _sb, 1, RD); }

  BAR; SCHED;
  STAMPK(1, 1);
  G2_RDK(aX, bX, lds0, 0); G2_RDK(aX, bX, lds0, 1);
  const int nh = isF ? 56 - G2_N0 : G2_N0;
  G2_STEP(0, aX, bX, aY, bY, true, GST4_SC1(odst, ov[0]));
  G2_STEP(1, aY, bY, aX, bX, true, GST4_SC1(odst + 1024, ov[1]));
  G2_STEP(2, aX, bX, aY, bY, true, GST4_SC1(odst + 2048, ov[2]));
  G2_STEP(3, aY, bY, aX, bX, true, GST4_SC1(odst + 3072, ov[3]));
  int t = 4;
  for (; t < 8; t += 2) {
    G2_STEP(t, aX, bX, aY, bY, true, (void)0);
    G2_STEP(t + 1, aY, bY, aX, bX, true, (void)0);
  }
  WAIT_V(0);
  G2_STEP(t, aX, bX, aY, bY, true, (void)0);
  if (threadIdx.x == 0) __hip_atomic_fetch_add((unsigned*)(ws + WS_CNT + (b >> 5) * 256), 1u, __ATOMIC_RELAXED, __HIP_MEMORY_SCOPE_AGENT);
  G2_STEP(t + 1, aY, bY, aX, bX, true, (void)0);
  t += 2;
  for (; t < nh - (isF ? 0 : 2); t += 2) {
    G2_STEP(t, aX, bX, aY, bY, true, (void)0);
    G2_STEP(t + 1, aY, bY, aX, bX, true, (void)0);
  }
  STAMPK(1, 2);
  if (isF) {
#pragma unroll
    for (int m = 0; m < 4; ++m)
#pragma unroll
      for (int j = 0; j < 4; ++j) acc[m][j] *= og[m];
    for (; t < n - 2; t += 2) {
      G2_STEP(t, aX, bX, aY, bY, true, (void)0);
      G2_STEP(t + 1, aY, bY, aX, bX, true, (void)0);
    }
  }
  G2_STEP(t, aX, bX, aY, bY, true, (void)0);
  G2_STEP(t + 1, aY, bY, aX, bX, false, (void)0);
  STAMPK(1, 3);
  if (!isF) {
#pragma unroll
    for (int m = 0; m < 4; ++m)
#pragma unroll
      for (int j = 0; j < 4; ++j) { f32x4 v = acc[m][j] * og[m]; GST4_SC1(part + wave * 16384 + (m * 4 + j) * 1024 + l * 16, v); }
    WAIT_V(0); BAR;
    STAMPK(1, 4);
  } else {
    BAR; SCHED;
    STAMPK(1, 4);
    const unsigned pa = lds0 + (unsigned)((((n - 4) + (wave >> 1)) & 3) * G2_STAGE + (wave & 1) * 16384 + l * 16);
#pragma unroll
    for (int m = 0; m < 4; ++m) {
      f32x4 p0, p1, p2, p3;
      ARD128(p0, pa, (m * 4 + 0) * 1024); ARD128(p1, pa, (m * 4 + 1) * 1024); ARD128(p2, pa, (m * 4 + 2) * 1024); ARD128(p3, pa, (m * 4 + 3) * 1024);
      WAIT_L(0); SCHED;
      f32x4 v0 = acc[m][0] + p0, v1 = acc[m][1] + p1, v2 = acc[m][2] + p2, v3 = acc[m][3] + p3;
      __bf16* dst = out + (size_t)(rt * 128 + wm * 64 + m * 16 + (l & 15)) * D_ + ctile * 128 + wn * 64 + (g4 & 1) * 16 + (g4 >> 1) * 8;
      u32x2 x0 = __builtin_amdgcn_permlane16_swap(pack2(v0[0], v0[1]), pack2(v1[0], v1[1]), false, false);
      u32x2 x1 = __builtin_amdgcn_permlane16_swap(pack2(v0[2], v0[3]), pack2(v1[2], v1[3]), false, false);
      u32x2 y0 = __builtin_amdgcn_permlane16_swap(pack2(v2[0], v2[1]), pack2(v3[0], v3[1]), false, false);
      u32x2 y1 = __builtin_amdgcn_permlane16_swap(pack2(v2[2], v2[3]), pack2(v3[2], v3[3]), false, false);
      *(u32x4*)(dst) = u32x4{x0[0], x1[0], x0[1], x1[1]};
      *(u32x4*)(dst + 32) = u32x4{y0[0], y1[0], y0[1], y1[1]};
    }
  }
  STAMPK(1, 5);
}

extern "C" void kernel_launch(void* const* d_in, const int* in_sizes, int n_in, void* d_out, int out_size, void* d_ws,
                              size_t ws_size, hipStream_t stream) {
  const __bf16* x = (const __bf16*)d_in[0];
  const __bf16* wqkv = (const __bf16*)d_in[1];
  const __bf16* wo = (const __bf16*)d_in[2];
  const __bf16* wg = (const __bf16*)d_in[4];
  const __bf16* wu = (const __bf16*)d_in[5];
  const __bf16* wd = (const __bf16*)d_in[6];
  const __bf16* gw = (const __bf16*)d_in[7];
  hipFuncSetAttribute((const void*)gemm1_kernel, hipFuncAttributeMaxDynamicSharedMemorySize, G1_LDS);
  hipFuncSetAttribute((const void*)gemm2_kernel, hipFuncAttributeMaxDynamicSharedMemorySize, G2_LDS);
  gemm1_kernel<<<256, 512, G1_LDS, stream>>>(x, wqkv, wg, wu, gw, (char*)d_ws, wd, wo);
  gemm2_kernel<<<256, 512, G2_LDS, stream>>>(wo, wd, (char*)d_ws, (__bf16*)d_out);
}
```
